```python
import torch

def _softmax(x, axis=-1):
    return torch.softmax(x, dim=axis)
def _gelu(x):
    return torch.nn.functional.gelu(x, approximate="tanh")
def _silu(x):
    return torch.nn.functional.silu(x)
def _sigmoid(x):
    return torch.sigmoid(x)
def _layernorm(x, axis=-1, eps=1e-5):
    m = x.mean(dim=axis, keepdim=True)
    v = x.var(dim=axis, keepdim=True, unbiased=False)
    return (x - m) / torch.sqrt(v + eps)
def _scatter_add(tgt, idx, src, axis=0):
    return tgt.clone().index_add_(axis, idx, src)
def _segment_sum(src, seg, n):
    out = torch.zeros((n,) + src.shape[1:], dtype=src.dtype)
    return out.index_add_(0, seg, src)
def _pad(x, pw):
    flat = tuple(v for pair in reversed(pw) for v in pair)
    return torch.nn.functional.pad(x, flat)

kernel_name = 'standalone__linear_attn_block__prompt'

def setup_inputs():

    _w = (lambda a: (lambda t: t.to(torch.bfloat16) if t.is_floating_point() else t)(torch.as_tensor(a)))
    B, S, D = 1, 2048, 640
    return {
        "x": _w((lambda *s: torch.randn(*s, dtype=torch.bfloat16) * 0.05)(B, S, D)),
        "wqkv": _w((lambda *s: torch.randn(*s, dtype=torch.bfloat16) * 0.4)(D, 3 * D)),
        "wo": _w((lambda *s: torch.randn(*s, dtype=torch.bfloat16) * 0.05)(D, D)),
    }

def reference(x, wqkv, wo):
    B, S, D = x.shape
    H = 32; Dh = D // H
    qkv = (lambda x, *d: x.permute(*d))((x @ wqkv).reshape(B, S, 3, H, Dh), 2, 0, 3, 1, 4)
    q, k, v = qkv[0], qkv[1], qkv[2]
    phi = lambda t: torch.where(t > 0, t + 1, torch.exp(t)) + 1e-6
    pq, pk = phi(q), phi(k)
    kv = torch.einsum("bhsd,bhse->bhde", pk, v)
    z = torch.einsum("bhsd->bhd", pk)
    num = torch.einsum("bhqd,bhde->bhqe", pq, kv)
    den = torch.einsum("bhqd,bhd->bhq", pq, z)[..., None] + 1e-6
    o = num / den
    return (lambda x, *d: x.permute(*d))(o, 0, 2, 1, 3).reshape(B, S, D) @ wo
```

```cpp
#include <hip/hip_runtime.h>
#include <hip/hip_bf16.h>
#include <stdint.h>

typedef __attribute__((ext_vector_type(8))) short bf16x8;
typedef __attribute__((ext_vector_type(4))) short s16x4;
typedef __attribute__((ext_vector_type(4))) float f32x4;
typedef __attribute__((address_space(3))) s16x4 lds_s16x4;
typedef unsigned short u16;

#define DI __device__ __forceinline__
#define MFMA16(a, b, c) __builtin_amdgcn_mfma_f32_16x16x32_bf16((a), (b), (c), 0, 0, 0)

constexpr int S_ = 2048, D_ = 640, N3 = 1920, NH = 32, DH = 20;

DI u16 f2bf(float f) { __bf16 b = (__bf16)f; return __builtin_bit_cast(u16, b); }
DI float bf2f(u16 u) { return __uint_as_float(((unsigned)u) << 16); }
DI float rbf(float f) { return bf2f(f2bf(f)); }
DI float phi_f(float acc) {
  float t = rbf(acc);
  float a = t > 0.f ? rbf(t + 1.0f) : rbf(__expf(t));
  return rbf(a + 1e-6f);
}
DI s16x4 tr_read(const char* p) {
  return __builtin_amdgcn_ds_read_tr16_b64_v4i16((lds_s16x4*)(p));
}
DI void glds16(const void* g, char* l) {
  __builtin_amdgcn_global_load_lds((const unsigned*)g, (unsigned*)l, 16, 0, 0);
}

constexpr int K1_AB = 128 * 64 * 2;
constexpr int K1_BB = 64 * 160 * 2;
constexpr int K1_ST = K1_AB + K1_BB;
constexpr int K1_NBUF = 4;
constexpr int K1_NSTEP = D_ / 64;

template <bool ISQ>
DI void k1_body(const u16* __restrict__ x, const u16* __restrict__ w, u16* __restrict__ pq,
                float* __restrict__ part, int rt, int cg, char* smem) {
  const int tid = threadIdx.x, lane = tid & 63, wave = tid >> 6;
  const int wr = wave >> 1, wc = wave & 1, fr = lane & 15, g = lane >> 4;
  const int r0 = rt * 128;

  const u16* gA[4];
  const u16* gB[5];
#pragma unroll
  for (int it = 0; it < 4; ++it) {
    int row = it * 32 + (tid >> 3);
    int c = (tid & 7) ^ (row & 7);
    gA[it] = x + (size_t)(r0 + row) * D_ + c * 8;
  }
#pragma unroll
  for (int it = 0; it < 5; ++it) {
    int i = it * 256 + tid;
    int half = i & 1, rp = (i >> 1) & 7, tile = i >> 4;
    int kb = tile / 10, nt = tile - kb * 10;
    int krow = kb * 8 + (rp ^ (4 * (kb & 1)));
    int c = 2 * nt + half;
    int col;
    if (ISQ) col = 160 * cg + 8 * c;
    else { int seg = c / 5, wi = c - seg * 5; col = ((seg & 1) ? 1280 : 640) + 80 * cg + (seg >> 1) * 40 + 8 * wi; }
    gB[it] = w + (size_t)krow * N3 + col;
  }
  auto stage = [&](int t) {
    char* base = smem + (t & (K1_NBUF - 1)) * K1_ST + wave * 1024;
#pragma unroll
    for (int it = 0; it < 4; ++it) glds16(gA[it] + t * 64, base + it * 4096);
#pragma unroll
    for (int it = 0; it < 5; ++it) glds16(gB[it] + (size_t)t * 64 * N3, base + K1_AB + it * 4096);
  };

  f32x4 acc[4][5];
#pragma unroll
  for (int m = 0; m < 4; ++m)
#pragma unroll
    for (int n = 0; n < 5; ++n) acc[m][n] = f32x4{0.f, 0.f, 0.f, 0.f};

  const int q4 = fr >> 2, p4 = fr & 3;
  const int aoff0 = (wr * 64 + fr) * 128 + ((g ^ (fr & 7)) * 16);
  const int aoff1 = (wr * 64 + fr) * 128 + (((4 + g) ^ (fr & 7)) * 16);
  const int boff0 = K1_AB + (g * 10 + wc * 5) * 256 + ((q4 ^ (4 * (g & 1))) * 32) + 8 * p4;
  const int boff1 = K1_AB + (g * 10 + wc * 5) * 256 + (((4 + q4) ^ (4 * (g & 1))) * 32) + 8 * p4;

  stage(0); stage(1); stage(2);
#pragma unroll
  for (int t = 0; t < K1_NSTEP; ++t) {
    if (t <= K1_NSTEP - 3) asm volatile("s_waitcnt vmcnt(18)" ::: "memory");
    else if (t == K1_NSTEP - 2) asm volatile("s_waitcnt vmcnt(9)" ::: "memory");
    else asm volatile("s_waitcnt vmcnt(0)" ::: "memory");
    __builtin_amdgcn_s_barrier();
    if (t + 3 < K1_NSTEP) stage(t + 3);
    const char* buf = smem + (t & (K1_NBUF - 1)) * K1_ST;
#pragma unroll
    for (int kk = 0; kk < 2; ++kk) {
      bf16x8 a[4], b[5];
#pragma unroll
      for (int m = 0; m < 4; ++m) a[m] = *(const bf16x8*)(buf + (kk ? aoff1 : aoff0) + m * 2048);
#pragma unroll
      for (int n = 0; n < 5; ++n) {
        s16x4 lo = tr_read(buf + boff0 + kk * 40 * 256 + n * 256);
        s16x4 hi = tr_read(buf + boff1 + kk * 40 * 256 + n * 256);
        b[n] = __builtin_shufflevector(lo, hi, 0, 1, 2, 3, 4, 5, 6, 7);
      }
#pragma unroll
      for (int m = 0; m < 4; ++m)
#pragma unroll
        for (int n = 0; n < 5; ++n) {
          if (ISQ) acc[m][n] = MFMA16(b[n], a[m], acc[m][n]);
          else acc[m][n] = MFMA16(a[m], b[n], acc[m][n]);
        }
    }
  }

  if (ISQ) {
#pragma unroll
    for (int m = 0; m < 4; ++m) {
      u16* dst = pq + (size_t)(r0 + wr * 64 + m * 16 + fr) * D_ + 160 * cg + wc * 80 + 4 * g;
#pragma unroll
      for (int n = 0; n < 5; ++n) {
        s16x4 v;
#pragma unroll
        for (int r = 0; r < 4; ++r) v[r] = (short)f2bf(phi_f(acc[m][n][r]));
        *(s16x4*)(dst + 16 * n) = v;
      }
    }
  } else {
    bf16x8 fr_[5][2];
    float zs[3] = {0.f, 0.f, 0.f};
#pragma unroll
    for (int n = 0; n < 5; ++n) {
      const bool isk = (16 * n + fr) < 40;
#pragma unroll
      for (int m = 0; m < 4; ++m)
#pragma unroll
        for (int r = 0; r < 4; ++r) {
          float v = isk ? phi_f(acc[m][n][r]) : rbf(acc[m][n][r]);
          if (n < 3) zs[n] += v;
          fr_[n][m >> 1][(m & 1) * 4 + r] = (short)f2bf(v);
        }
    }
    constexpr int PA[7] = {0, 0, 1, 1, 1, 2, 2};
    constexpr int PB[7] = {2, 3, 2, 3, 4, 3, 4};
    float* pbase = part + (size_t)((rt * 2 + wr) * NH + cg * 4 + wc * 2) * 420;
#pragma unroll
    for (int pi = 0; pi < 7; ++pi) {
      f32x4 kv = f32x4{0.f, 0.f, 0.f, 0.f};
      kv = MFMA16(fr_[PA[pi]][0], fr_[PB[pi]][0], kv);
      kv = MFMA16(fr_[PA[pi]][1], fr_[PB[pi]][1], kv);
      const int ecol = 16 * PB[pi] + fr - 40;
#pragma unroll
      for (int r = 0; r < 4; ++r) {
        const int dcol = 16 * PA[pi] + 4 * g + r;
        if (dcol < 40 && ecol >= 0) {
          const int hd = dcol >= 20, he = ecol >= 20;
          if (hd == he) pbase[hd * 420 + (dcol - 20 * hd) * 20 + (ecol - 20 * he)] = kv[r];
        }
      }
    }
#pragma unroll
    for (int n = 0; n < 3; ++n) {
      float z = zs[n];
      z += __shfl_xor(z, 16);
      z += __shfl_xor(z, 32);
      const int dcol = 16 * n + fr;
      if (g == 0 && dcol < 40) {
        const int hd = dcol >= 20;
        pbase[hd * 420 + 400 + dcol - 20 * hd] = z;
      }
    }
  }
}

__global__ __launch_bounds__(256, 1) void k1_qkv(const u16* __restrict__ x, const u16* __restrict__ w,
                                                 u16* __restrict__ pq, float* __restrict__ part) {
  __shared__ __attribute__((aligned(16))) char smem[K1_NBUF * K1_ST];
  const int bid = blockIdx.x, xcd = bid & 7, loc = bid >> 3;
  const int rt = (xcd & 3) * 4 + (loc & 3);
  const int cgi = (xcd >> 2) * 6 + (loc >> 2);
  if (cgi < 8) k1_body<false>(x, w, pq, part, rt, cgi, smem);
  else k1_body<true>(x, w, pq, part, rt, cgi - 8, smem);
}

constexpr int NPART = 32;
__global__ __launch_bounds__(256) void k2_o(const u16* __restrict__ pq, const float* __restrict__ part,
                                            u16* __restrict__ o) {
  __shared__ __attribute__((aligned(16))) u16 T[32 * 32];
  const int tid = threadIdx.x, lane = tid & 63, wave = tid >> 6, fr = lane & 15, g = lane >> 4;
  const int h = blockIdx.x & 31, rc = blockIdx.x >> 5;
  ((unsigned*)T)[tid] = 0u; ((unsigned*)T)[tid + 256] = 0u;
  __syncthreads();
#pragma unroll
  for (int rep = 0; rep < 2; ++rep) {
    const int idx = rep * 256 + tid;
    if (idx < 420) {
      float s = 0.f;
#pragma unroll
      for (int p = 0; p < NPART; ++p) s += part[(size_t)(p * NH + h) * 420 + idx];
      int e, d;
      if (idx < 400) { d = idx / 20; e = idx - d * 20; } else { d = idx - 400; e = 20; }
      T[e * 32 + d] = f2bf(s);
    }
  }
  __syncthreads();
  const bf16x8 t0 = *(const bf16x8*)(T + fr * 32 + 8 * g);
  const bf16x8 t1 = *(const bf16x8*)(T + (16 + fr) * 32 + 8 * g);
#pragma unroll
  for (int u = 0; u < 4; ++u) {
    const int row = rc * 256 + wave * 64 + u * 16 + fr;
    const u16* src = pq + (size_t)row * D_ + h * DH + 8 * g;
    bf16x8 b = bf16x8{0, 0, 0, 0, 0, 0, 0, 0};
    if (g < 2) {
      s16x4 lo = *(const s16x4*)(src), hi = *(const s16x4*)(src + 4);
      b = __builtin_shufflevector(lo, hi, 0, 1, 2, 3, 4, 5, 6, 7);
    } else if (g == 2) {
      s16x4 lo = *(const s16x4*)(src);
      b[0] = lo[0]; b[1] = lo[1]; b[2] = lo[2]; b[3] = lo[3];
    }
    f32x4 z4 = f32x4{0.f, 0.f, 0.f, 0.f};
    f32x4 n0 = MFMA16(t0, b, z4);
    f32x4 n1 = MFMA16(t1, b, z4);
    float den = __shfl(n1[0], 16 + fr);
    den = rbf(rbf(den) + 1e-6f);
    u16* dst = o + (size_t)row * D_ + h * DH;
    s16x4 v0, v1;
#pragma unroll
    for (int r = 0; r < 4; ++r) {
      v0[r] = (short)f2bf(rbf(n0[r]) / den);
      v1[r] = (short)f2bf(rbf(n1[r]) / den);
    }
    *(s16x4*)(dst + 4 * g) = v0;
    if (g == 0) *(s16x4*)(dst + 16) = v1;
  }
}

__global__ __launch_bounds__(256, 1) void k3_out(const u16* __restrict__ o, const u16* __restrict__ wo,
                                                 u16* __restrict__ out) {
  __shared__ __attribute__((aligned(16))) char smem[640 * 80 * 2];
  const int tid = threadIdx.x, lane = tid & 63, wave = tid >> 6, fr = lane & 15, g = lane >> 4;
  const int bid = blockIdx.x, xcd = bid & 7, loc = bid >> 3;
  const int rt = xcd * 4 + (loc & 3), ct = loc >> 2;
  const int r0 = rt * 64, n0 = ct * 80;
#pragma unroll
  for (int it = 0; it < 25; ++it) {
    int i = it * 256 + tid;
    int half = i & 1, rp = (i >> 1) & 7, tile = i >> 4;
    int kb = tile / 5, nt = tile - kb * 5;
    int krow = kb * 8 + (rp ^ (4 * (kb & 1)));
    glds16(wo + (size_t)krow * D_ + n0 + nt * 16 + half * 8, smem + (it * 256 + wave * 64) * 16);
  }
  bf16x8 a[20];
  const u16* asrc = o + (size_t)(r0 + wave * 16 + fr) * D_ + 8 * g;
#pragma unroll
  for (int ks = 0; ks < 20; ++ks) a[ks] = *(const bf16x8*)(asrc + ks * 32);
  asm volatile("s_waitcnt vmcnt(0)" ::: "memory");
  __builtin_amdgcn_s_barrier();
  const int q4 = fr >> 2, p4 = fr & 3;
  const int boff0 = g * 5 * 256 + ((q4 ^ (4 * (g & 1))) * 32) + 8 * p4;
  const int boff1 = g * 5 * 256 + (((4 + q4) ^ (4 * (g & 1))) * 32) + 8 * p4;
  f32x4 acc[5];
#pragma unroll
  for (int n = 0; n < 5; ++n) acc[n] = f32x4{0.f, 0.f, 0.f, 0.f};
#pragma unroll
  for (int ks = 0; ks < 20; ++ks) {
#pragma unroll
    for (int n = 0; n < 5; ++n) {
      s16x4 lo = tr_read(smem + boff0 + ks * 20 * 256 + n * 256);
      s16x4 hi = tr_read(smem + boff1 + ks * 20 * 256 + n * 256);
      bf16x8 b = __builtin_shufflevector(lo, hi, 0, 1, 2, 3, 4, 5, 6, 7);
      acc[n] = MFMA16(b, a[ks], acc[n]);
    }
  }
  u16* dst = out + (size_t)(r0 + wave * 16 + fr) * D_ + n0 + 4 * g;
#pragma unroll
  for (int n = 0; n < 5; ++n) {
    s16x4 v;
#pragma unroll
    for (int r = 0; r < 4; ++r) v[r] = (short)f2bf(acc[n][r]);
    *(s16x4*)(dst + 16 * n) = v;
  }
}

extern "C" void kernel_launch(void* const* d_in, const int* in_sizes, int n_in,
                              void* d_out, int out_size, void* d_ws, size_t ws_size,
                              hipStream_t stream) {
  const u16* x = (const u16*)d_in[0];
  const u16* w = (const u16*)d_in[1];
  const u16* wo = (const u16*)d_in[2];
  char* ws = (char*)d_ws;
  u16* pq = (u16*)ws;
  u16* o = (u16*)(ws + 2621440);
  float* part = (float*)(ws + 2 * 2621440);
  k1_qkv<<<192, 256, 0, stream>>>(x, w, pq, part);
  k2_o<<<256, 256, 0, stream>>>(pq, part, o);
  k3_out<<<256, 256, 0, stream>>>(o, wo, (u16*)d_out);
}
```

```cpp
#include <hip/hip_runtime.h>
#include <hip/hip_bf16.h>
#include <stdint.h>

typedef __attribute__((ext_vector_type(8))) short bf16x8;
typedef __attribute__((ext_vector_type(4))) short s16x4;
typedef __attribute__((ext_vector_type(4))) float f32x4;
typedef __attribute__((address_space(3))) s16x4 lds_s16x4;
typedef unsigned short u16;

#define DI __device__ __forceinline__
#define MFMA16(a, b, c) __builtin_amdgcn_mfma_f32_16x16x32_bf16((a), (b), (c), 0, 0, 0)
#define STAMP(i)

constexpr int S_ = 2048, D_ = 640, N3 = 1920, NH = 32, DH = 20;

DI u16 f2bf(float f) { __bf16 b = (__bf16)f; return __builtin_bit_cast(u16, b); }
DI float bf2f(u16 u) { return __uint_as_float(((unsigned)u) << 16); }
DI float rbf(float f) { return bf2f(f2bf(f)); }
DI float phi_f(float acc) {
  float t = rbf(acc);
  float a = t > 0.f ? (t + 1.0f) : __expf(t);
  return rbf(rbf(a) + 1e-6f);
}
DI s16x4 tr_read(const char* p) {
  return __builtin_amdgcn_ds_read_tr16_b64_v4i16((lds_s16x4*)(p));
}
DI void glds16(const void* g, char* l) {
  __builtin_amdgcn_global_load_lds((const unsigned*)g, (unsigned*)l, 16, 0, 0);
}
#define WAITV(n) asm volatile("s_waitcnt vmcnt(" #n ")" ::: "memory")

constexpr int K1_AB = 128 * 64 * 2;
constexpr int K1_BB = 64 * 160 * 2;
constexpr int K1_ST = K1_AB + K1_BB;
constexpr int K1_NBUF = 4;
constexpr int K1_NSTEP = D_ / 64;
constexpr int NPART = 16;

template <bool ISQ>
DI void k1_body(const u16* __restrict__ x, const u16* __restrict__ w, u16* __restrict__ pq,
                float* __restrict__ part, int rt, int cg, char* smem) {
  const int tid = threadIdx.x, lane = tid & 63;
  const int wave = __builtin_amdgcn_readfirstlane(tid >> 6);
  const int wr = wave >> 1, wc = wave & 1, fr = lane & 15, g = lane >> 4;
  const int r0 = rt * 128;
  const bool aw = wave < 4;

  const u16* gp[5];
  if (aw) {
#pragma unroll
    for (int it = 0; it < 4; ++it) {
      int i = (it * 4 + wave) * 64 + lane;
      int row = i >> 3;
      int c = (i & 7) ^ (row & 7);
      gp[it] = x + (size_t)(r0 + row) * D_ + c * 8;
    }
    gp[4] = gp[3];
  } else {
#pragma unroll
    for (int it = 0; it < 5; ++it) {
      int i = (it * 4 + wave - 4) * 64 + lane;
      int half = i & 1, rp = (i >> 1) & 7, tile = i >> 4;
      int kb = tile / 10, nt = tile - kb * 10;
      int krow = kb * 8 + (rp ^ (4 * (kb & 1)));
      int c = 2 * nt + half;
      int col;
      if (ISQ) col = 160 * cg + 8 * c;
      else { int seg = c / 5, wi = c - seg * 5; col = ((seg & 1) ? 1280 : 640) + 80 * cg + (seg >> 1) * 40 + 8 * wi; }
      gp[it] = w + (size_t)krow * N3 + col;
    }
  }
  const size_t kstride = aw ? 64 : (size_t)64 * N3;
  const int lbase = aw ? wave * 1024 : K1_AB + (wave - 4) * 1024;
  auto stage = [&](int t) {
    char* base = smem + (t & (K1_NBUF - 1)) * K1_ST + lbase;
#pragma unroll
    for (int it = 0; it < 4; ++it) glds16(gp[it] + t * kstride, base + it * 4096);
    if (!aw) glds16(gp[4] + t * kstride, base + 4 * 4096);
  };

  f32x4 acc[2][5];
#pragma unroll
  for (int m = 0; m < 2; ++m)
#pragma unroll
    for (int n = 0; n < 5; ++n) acc[m][n] = f32x4{0.f, 0.f, 0.f, 0.f};

  const int q4 = fr >> 2, p4 = fr & 3;
  const int aoff0 = (wr * 32 + fr) * 128 + ((g ^ (fr & 7)) * 16);
  const int aoff1 = (wr * 32 + fr) * 128 + (((4 + g) ^ (fr & 7)) * 16);
  const int boff0 = K1_AB + (g * 10 + wc * 5) * 256 + ((q4 ^ (4 * (g & 1))) * 32) + 8 * p4;
  const int boff1 = K1_AB + (g * 10 + wc * 5) * 256 + (((4 + q4) ^ (4 * (g & 1))) * 32) + 8 * p4;

  STAMP(0);
  stage(0); stage(1); stage(2);
  STAMP(1);
#pragma unroll
  for (int t = 0; t < K1_NSTEP; ++t) {
    if (aw) {
      if (t <= K1_NSTEP - 3) WAITV(8);
      else if (t == K1_NSTEP - 2) WAITV(4);
      else WAITV(0);
    } else {
      if (t <= K1_NSTEP - 3) WAITV(10);
      else if (t == K1_NSTEP - 2) WAITV(5);
      else WAITV(0);
    }
    __builtin_amdgcn_s_barrier();
    if (t == 0) STAMP(2);
    if (t == 5) STAMP(3);
    if (t + 3 < K1_NSTEP) stage(t + 3);
    const char* buf = smem + (t & (K1_NBUF - 1)) * K1_ST;
#pragma unroll
    for (int kk = 0; kk < 2; ++kk) {
      bf16x8 a[2], b[5];
#pragma unroll
      for (int m = 0; m < 2; ++m) a[m] = *(const bf16x8*)(buf + (kk ? aoff1 : aoff0) + m * 2048);
#pragma unroll
      for (int n = 0; n < 5; ++n) {
        s16x4 lo = tr_read(buf + boff0 + kk * 40 * 256 + n * 256);
        s16x4 hi = tr_read(buf + boff1 + kk * 40 * 256 + n * 256);
        b[n] = __builtin_shufflevector(lo, hi, 0, 1, 2, 3, 4, 5, 6, 7);
      }
#pragma unroll
      for (int m = 0; m < 2; ++m)
#pragma unroll
        for (int n = 0; n < 5; ++n) {
          if (ISQ) acc[m][n] = MFMA16(b[n], a[m], acc[m][n]);
          else acc[m][n] = MFMA16(a[m], b[n], acc[m][n]);
        }
    }
  }
  STAMP(4);
  char* ep = smem + 2 * K1_ST;
  if (ISQ) {
    constexpr int RS = 336;
#pragma unroll
    for (int m = 0; m < 2; ++m) {
      char* dst = ep + (wr * 32 + m * 16 + fr) * RS + (wc * 80 + 4 * g) * 2;
#pragma unroll
      for (int n = 0; n < 5; ++n) {
        s16x4 v;
#pragma unroll
        for (int r = 0; r < 4; ++r) v[r] = (short)f2bf(phi_f(acc[m][n][r]));
        *(s16x4*)(dst + 32 * n) = v;
      }
    }
    __syncthreads();
#pragma unroll
    for (int it = 0; it < 5; ++it) {
      int i = it * 512 + tid;
      int row = i / 20, c = i - row * 20;
      bf16x8 v = *(const bf16x8*)(ep + row * RS + c * 16);
      *(bf16x8*)(pq + (size_t)(r0 + row) * D_ + 160 * cg + c * 8) = v;
    }
  } else {
    bf16x8 fr_[5];
    float zs[3] = {0.f, 0.f, 0.f};
#pragma unroll
    for (int n = 0; n < 5; ++n) {
      const bool isk = (16 * n + fr) < 40;
#pragma unroll
      for (int m = 0; m < 2; ++m)
#pragma unroll
        for (int r = 0; r < 4; ++r) {
          float v;
          if (n < 2) v = phi_f(acc[m][n][r]);
          else if (n == 2) v = isk ? phi_f(acc[m][n][r]) : rbf(acc[m][n][r]);
          else v = rbf(acc[m][n][r]);
          if (n < 3) zs[n] += v;
          fr_[n][m * 4 + r] = (short)f2bf(v);
        }
    }
    constexpr int PA[7] = {0, 0, 1, 1, 1, 2, 2};
    constexpr int PB[7] = {2, 3, 2, 3, 4, 3, 4};
    float* pl = (float*)ep + wave * 840;
#pragma unroll
    for (int pi = 0; pi < 7; ++pi) {
      f32x4 kv = f32x4{0.f, 0.f, 0.f, 0.f};
      kv = MFMA16(fr_[PA[pi]], fr_[PB[pi]], kv);
      const int ecol = 16 * PB[pi] + fr - 40;
#pragma unroll
      for (int r = 0; r < 4; ++r) {
        const int dcol = 16 * PA[pi] + 4 * g + r;
        if (dcol < 40 && ecol >= 0) {
          const int hd = dcol >= 20, he = ecol >= 20;
          if (hd == he) pl[hd * 420 + (dcol - 20 * hd) * 20 + (ecol - 20 * he)] = kv[r];
        }
      }
    }
#pragma unroll
    for (int n = 0; n < 3; ++n) {
      float z = zs[n];
      z += __shfl_xor(z, 16);
      z += __shfl_xor(z, 32);
      const int dcol = 16 * n + fr;
      if (g == 0 && dcol < 40) {
        const int hd = dcol >= 20;
        pl[hd * 420 + 400 + dcol - 20 * hd] = z;
      }
    }
    __syncthreads();
    if (tid < 420) {
      const int wcs = tid >= 210, i4 = tid - 210 * wcs;
      f32x4 s = f32x4{0.f, 0.f, 0.f, 0.f};
#pragma unroll
      for (int r = 0; r < 4; ++r) s += *(const f32x4*)((const float*)ep + (r * 2 + wcs) * 840 + i4 * 4);
      *(f32x4*)(part + (size_t)(rt * NH + cg * 4) * 420 + tid * 4) = s;
    }
  }
}

__global__ __launch_bounds__(512, 2) void k1_qkv(const u16* __restrict__ x, const u16* __restrict__ w,
                                                 u16* __restrict__ pq, float* __restrict__ part) {
  __shared__ __attribute__((aligned(16))) char smem[K1_NBUF * K1_ST];
  const int bid = blockIdx.x, xcd = bid & 7, loc = bid >> 3;
  const int rt = (xcd & 3) * 4 + (loc & 3);
  const int cgi = (xcd >> 2) * 6 + (loc >> 2);
  if (cgi < 8) k1_body<false>(x, w, pq, part, rt, cgi, smem);
  else k1_body<true>(x, w, pq, part, rt, cgi - 8, smem);
}

__global__ __launch_bounds__(256) void k2_red(const float* __restrict__ part, u16* __restrict__ Tf) {
  __shared__ float red[420];
  const int tid = threadIdx.x, h = blockIdx.x;
#pragma unroll
  for (int rep = 0; rep < 2; ++rep) {
    const int idx = rep * 256 + tid;
    if (idx < 420) {
      float s = 0.f;
#pragma unroll
      for (int p = 0; p < NPART; ++p) s += part[(size_t)(p * NH + h) * 420 + idx];
      red[idx] = s;
    }
  }
  __syncthreads();
  if (tid < 128) {
    const int tile = tid >> 6, lane = tid & 63, fr = lane & 15, g = lane >> 4;
    const int c = fr >> 2, r = fr & 3;
    bf16x8 v;
#pragma unroll
    for (int j = 0; j < 8; ++j) {
      const int d = 8 * g + j;
      float val = 0.f;
      if (d < 20) {
        if (tile == 0) val = red[d * 20 + 5 * c + r];
        else if (r == 0) val = red[d * 20 + 5 * c + 4];
        else if (r == 1) val = red[400 + d];
      }
      v[j] = (short)f2bf(val);
    }
    *(bf16x8*)(Tf + (size_t)(h * 128 + tid) * 8) = v;
  }
}

__global__ __launch_bounds__(512, 2) void k3_out(const u16* __restrict__ pq, const u16* __restrict__ Tf,
                                                 const u16* __restrict__ wo, u16* __restrict__ out) {
  __shared__ __attribute__((aligned(16))) char smem[640 * 80 * 2 + 4 * 5 * 1024];
  const int tid = threadIdx.x, lane = tid & 63, fr = lane & 15, g = lane >> 4;
  const int wave = __builtin_amdgcn_readfirstlane(tid >> 6);
  const int rtile = wave & 3, hk = wave >> 2;
  const int bid = blockIdx.x, xcd = bid & 7, loc = bid >> 3;
  const int rt = xcd * 4 + (loc & 3), ct = loc >> 2;
  const int r0 = rt * 64, n0 = ct * 80;
  STAMP(0);
#pragma unroll
  for (int it = 0; it < 13; ++it) {
    int i = it * 512 + tid;
    if (it < 12 || wave < 4) {
      int half = i & 1, rp = (i >> 1) & 7, tile = i >> 4;
      int kb = tile / 5, nt = tile - kb * 5;
      int jj = rp ^ (4 * (kb & 1));
      int ks = kb >> 2, gg = kb & 3;
      int v = ks * 8 + jj;
      int head = v / 5, ii = v - head * 5;
      int krow = head * 20 + 5 * gg + ii;
      glds16(wo + (size_t)krow * D_ + n0 + nt * 16 + half * 8, smem + (it * 512 + wave * 64) * 16);
    }
  }
  STAMP(1);
  const u16* psrc = pq + (size_t)(r0 + rtile * 16 + fr) * D_ + hk * 320 + 8 * g;
  const u16* tsrc = Tf + (size_t)(hk * 16 * 128 + lane) * 8;
  unsigned ow[40];
#pragma unroll
  for (int i = 0; i < 40; ++i) ow[i] = 0u;
#pragma unroll
  for (int hb = 0; hb < 4; ++hb) {
    bf16x8 pb[4], t0[4], t1[4];
#pragma unroll
    for (int hh = 0; hh < 4; ++hh) {
      const int h = hb * 4 + hh;
      bf16x8 b = bf16x8{0, 0, 0, 0, 0, 0, 0, 0};
      if (g < 2) {
        s16x4 lo = *(const s16x4*)(psrc + h * 20), hi = *(const s16x4*)(psrc + h * 20 + 4);
        b = __builtin_shufflevector(lo, hi, 0, 1, 2, 3, 4, 5, 6, 7);
      } else if (g == 2) {
        s16x4 lo = *(const s16x4*)(psrc + h * 20);
        b[0] = lo[0]; b[1] = lo[1]; b[2] = lo[2]; b[3] = lo[3];
      }
      pb[hh] = b;
      t0[hh] = *(const bf16x8*)(tsrc + h * 1024);
      t1[hh] = *(const bf16x8*)(tsrc + h * 1024 + 512);
    }
#pragma unroll
    for (int hh = 0; hh < 4; ++hh) {
      const int h = hb * 4 + hh;
      f32x4 z4 = f32x4{0.f, 0.f, 0.f, 0.f};
      f32x4 n0v = MFMA16(t0[hh], pb[hh], z4);
      f32x4 n1v = MFMA16(t1[hh], pb[hh], z4);
      float den = rbf(rbf(n1v[1]) + 1e-6f);
      float rd = __builtin_amdgcn_rcpf(den);
#pragma unroll
      for (int r = 0; r < 4; ++r) ow[(h * 5 + r) >> 1] |= (unsigned)f2bf(rbf(n0v[r]) * rd) << (16 * ((h * 5 + r) & 1));
      ow[(h * 5 + 4) >> 1] |= (unsigned)f2bf(rbf(n1v[0]) * rd) << (16 * ((h * 5 + 4) & 1));
    }
  }
  STAMP(2);
  WAITV(0);
  __builtin_amdgcn_s_barrier();
  STAMP(3);
  const int q4 = fr >> 2, p4 = fr & 3;
  const int boff0 = (hk * 40 + g) * 5 * 256 + ((q4 ^ (4 * (g & 1))) * 32) + 8 * p4;
  const int boff1 = (hk * 40 + g) * 5 * 256 + (((4 + q4) ^ (4 * (g & 1))) * 32) + 8 * p4;
  f32x4 acc[5];
#pragma unroll
  for (int n = 0; n < 5; ++n) acc[n] = f32x4{0.f, 0.f, 0.f, 0.f};
#pragma unroll
  for (int ks = 0; ks < 10; ++ks) {
    typedef __attribute__((ext_vector_type(4))) unsigned u32x4;
    u32x4 aw4 = u32x4{ow[ks * 4], ow[ks * 4 + 1], ow[ks * 4 + 2], ow[ks * 4 + 3]};
    bf16x8 a = __builtin_bit_cast(bf16x8, aw4);
#pragma unroll
    for (int n = 0; n < 5; ++n) {
      s16x4 lo = tr_read(smem + boff0 + ks * 20 * 256 + n * 256);
      s16x4 hi = tr_read(smem + boff1 + ks * 20 * 256 + n * 256);
      bf16x8 b = __builtin_shufflevector(lo, hi, 0, 1, 2, 3, 4, 5, 6, 7);
      acc[n] = MFMA16(b, a, acc[n]);
    }
  }
  STAMP(4);
  f32x4* redb = (f32x4*)(smem + 640 * 80 * 2);
  if (hk == 1) {
#pragma unroll
    for (int n = 0; n < 5; ++n) redb[(rtile * 5 + n) * 64 + lane] = acc[n];
  }
  __syncthreads();
  if (hk == 0) {
    u16* dst = out + (size_t)(r0 + rtile * 16 + fr) * D_ + n0 + 4 * g;
#pragma unroll
    for (int n = 0; n < 5; ++n) {
      f32x4 s = acc[n] + redb[(rtile * 5 + n) * 64 + lane];
      s16x4 v;
#pragma unroll
      for (int r = 0; r < 4; ++r) v[r] = (short)f2bf(s[r]);
      *(s16x4*)(dst + 16 * n) = v;
    }
  }
}

extern "C" void kernel_launch(void* const* d_in, const int* in_sizes, int n_in,
                              void* d_out, int out_size, void* d_ws, size_t ws_size,
                              hipStream_t stream) {
  const u16* x = (const u16*)d_in[0];
  const u16* w = (const u16*)d_in[1];
  const u16* wo = (const u16*)d_in[2];
  char* ws = (char*)d_ws;
  u16* pq = (u16*)ws;
  float* part = (float*)(ws + 2621440);
  u16* Tf = (u16*)(ws + 2621440 + 860160);
  k1_qkv<<<192, 512, 0, stream>>>(x, w, pq, part);
  k2_red<<<32, 256, 0, stream>>>(part, Tf);
  k3_out<<<256, 512, 0, stream>>>(pq, Tf, wo, (u16*)d_out);
}
```

```cpp
#include <hip/hip_runtime.h>
#include <hip/hip_bf16.h>
#include <stdint.h>

typedef __attribute__((ext_vector_type(8))) short bf16x8;
typedef __attribute__((ext_vector_type(4))) short s16x4;
typedef __attribute__((ext_vector_type(4))) float f32x4;
typedef __attribute__((ext_vector_type(4))) unsigned u32x4;
typedef __attribute__((ext_vector_type(2))) unsigned u32x2;
typedef __attribute__((address_space(3))) s16x4 lds_s16x4;
typedef unsigned short u16;

#define DI __device__ __forceinline__
#define MFMA16(a, b, c) __builtin_amdgcn_mfma_f32_16x16x32_bf16((a), (b), (c), 0, 0, 0)
#define STAMPA(i)
#define STAMPB(i)

constexpr int S_ = 2048, D_ = 640, N3 = 1920, NH = 32, DH = 20;

DI u16 f2bf(float f) { __bf16 b = (__bf16)f; return __builtin_bit_cast(u16, b); }
DI float bf2f(u16 u) { return __uint_as_float(((unsigned)u) << 16); }
DI float rbf(float f) { return bf2f(f2bf(f)); }
DI unsigned pk2(float lo, float hi) { return (unsigned)f2bf(lo) | ((unsigned)f2bf(hi) << 16); }
DI float lo16(unsigned u) { return __uint_as_float(u << 16); }
DI float hi16(unsigned u) { return __uint_as_float(u & 0xffff0000u); }
DI float phi_t(float t) {
  float a = t > 0.f ? (t + 1.0f) : __expf(t);
  return rbf(a) + 1e-6f;
}
DI unsigned phi_pk(unsigned u) { return pk2(phi_t(lo16(u)), phi_t(hi16(u))); }
DI s16x4 tr_read(const char* p) {
  return __builtin_amdgcn_ds_read_tr16_b64_v4i16((lds_s16x4*)(p));
}
DI void glds16(const void* g, char* l) {
  __builtin_amdgcn_global_load_lds((const unsigned*)g, (unsigned*)l, 16, 0, 0);
}
#define WAITV(n) asm volatile("s_waitcnt vmcnt(" #n ")" ::: "memory")

constexpr int K1_AB = 128 * 64 * 2;
constexpr int K1_BB = 64 * 160 * 2;
constexpr int K1_ST = K1_AB + K1_BB;
constexpr int K1_NBUF = 4;
constexpr int K1_NSTEP = D_ / 64;
constexpr int NPART = 16;
constexpr int ERS = 352;

__global__ __launch_bounds__(512, 2) void k1_qkv(const u16* __restrict__ x, const u16* __restrict__ w,
                                                 u16* __restrict__ pq, float* __restrict__ part) {
  __shared__ __attribute__((aligned(16))) char smem[K1_NBUF * K1_ST];
  const int bid = blockIdx.x, xcd = bid & 7, loc = bid >> 3;
  const int rt = (xcd & 3) * 4 + (loc & 3);
  const int cgi = (xcd >> 2) * 6 + (loc >> 2);
  const bool isq = cgi >= 8;
  const int cg = isq ? cgi - 8 : cgi;

  const int tid = threadIdx.x, lane = tid & 63;
  const int wave = __builtin_amdgcn_readfirstlane(tid >> 6);
  const int wr = wave >> 1, wc = wave & 1, fr = lane & 15, g = lane >> 4;
  const int r0 = rt * 128;
  const bool aw = wave < 4;

  const u16* gp[5];
  if (aw) {
#pragma unroll
    for (int it = 0; it < 4; ++it) {
      int i = (it * 4 + wave) * 64 + lane;
      int row = i >> 3;
      int c = (i & 7) ^ (row & 7);
      gp[it] = x + (size_t)(r0 + row) * D_ + c * 8;
    }
    gp[4] = gp[3];
  } else {
#pragma unroll
    for (int it = 0; it < 5; ++it) {
      int i = (it * 4 + wave - 4) * 64 + lane;
      int half = i & 1, rp = (i >> 1) & 7, tile = i >> 4;
      int kb = tile / 10, nt = tile - kb * 10;
      int krow = kb * 8 + (rp ^ (4 * (kb & 1)));
      int c = 2 * nt + half;
      int seg = c / 5, wi = c - seg * 5;
      int col = isq ? (160 * cg + 8 * c) : (((seg & 1) ? 1280 : 640) + 80 * cg + (seg >> 1) * 40 + 8 * wi);
      gp[it] = w + (size_t)krow * N3 + col;
    }
  }
  const size_t kstride = aw ? 64 : (size_t)64 * N3;
  const int lbase = aw ? wave * 1024 : K1_AB + (wave - 4) * 1024;
  auto stage = [&](int t) {
    char* base = smem + (t & (K1_NBUF - 1)) * K1_ST + lbase;
#pragma unroll
    for (int it = 0; it < 4; ++it) glds16(gp[it] + t * kstride, base + it * 4096);
    if (!aw) glds16(gp[4] + t * kstride, base + 4 * 4096);
  };

  f32x4 acc[2][5];
#pragma unroll
  for (int m = 0; m < 2; ++m)
#pragma unroll
    for (int n = 0; n < 5; ++n) acc[m][n] = f32x4{0.f, 0.f, 0.f, 0.f};

  const int q4 = fr >> 2, p4 = fr & 3;
  const int aoff0 = (wr * 32 + fr) * 128 + ((g ^ (fr & 7)) * 16);
  const int aoff1 = (wr * 32 + fr) * 128 + (((4 + g) ^ (fr & 7)) * 16);
  const int boff0 = K1_AB + (g * 10 + wc * 5) * 256 + ((q4 ^ (4 * (g & 1))) * 32) + 8 * p4;
  const int boff1 = K1_AB + (g * 10 + wc * 5) * 256 + (((4 + q4) ^ (4 * (g & 1))) * 32) + 8 * p4;

  STAMPA(0);
  stage(0); stage(1); stage(2);
#pragma unroll
  for (int t = 0; t < K1_NSTEP; ++t) {
    if (aw) {
      if (t <= K1_NSTEP - 3) WAITV(8);
      else if (t == K1_NSTEP - 2) WAITV(4);
      else WAITV(0);
    } else {
      if (t <= K1_NSTEP - 3) WAITV(10);
      else if (t == K1_NSTEP - 2) WAITV(5);
      else WAITV(0);
    }
    __builtin_amdgcn_s_barrier();
    if (aw && t + 3 < K1_NSTEP) stage(t + 3);
    const char* buf = smem + (t & (K1_NBUF - 1)) * K1_ST;
#pragma unroll
    for (int kk = 0; kk < 2; ++kk) {
      bf16x8 a[2], b[5];
#pragma unroll
      for (int m = 0; m < 2; ++m) a[m] = *(const bf16x8*)(buf + (kk ? aoff1 : aoff0) + m * 2048);
#pragma unroll
      for (int n = 0; n < 5; ++n) {
        s16x4 lo = tr_read(buf + boff0 + kk * 40 * 256 + n * 256);
        s16x4 hi = tr_read(buf + boff1 + kk * 40 * 256 + n * 256);
        b[n] = __builtin_shufflevector(lo, hi, 0, 1, 2, 3, 4, 5, 6, 7);
      }
#pragma unroll
      for (int m = 0; m < 2; ++m)
#pragma unroll
        for (int n = 0; n < 5; ++n) acc[m][n] = MFMA16(b[n], a[m], acc[m][n]);
    }
    if (!aw && t + 3 < K1_NSTEP) stage(t + 3);
  }
  STAMPA(4);
  char* ep = smem + 2 * K1_ST;
#pragma unroll
  for (int m = 0; m < 2; ++m) {
    char* dst = ep + (wr * 32 + m * 16 + fr) * ERS + (wc * 80 + 4 * g) * 2;
#pragma unroll
    for (int n = 0; n < 5; ++n)
      *(u32x2*)(dst + 32 * n) = u32x2{pk2(acc[m][n][0], acc[m][n][1]), pk2(acc[m][n][2], acc[m][n][3])};
  }
  __syncthreads();
  STAMPA(1);
  if (isq) {
#pragma unroll 1
    for (int it = 0; it < 5; ++it) {
      const int i = it * 512 + tid;
      const int row = i / 20, c = i - row * 20;
      u32x4 v = *(const u32x4*)(ep + row * ERS + c * 16);
      v[0] = phi_pk(v[0]); v[1] = phi_pk(v[1]); v[2] = phi_pk(v[2]); v[3] = phi_pk(v[3]);
      *(u32x4*)(pq + (size_t)(r0 + row) * D_ + 160 * cg + c * 8) = v;
    }
  } else {
#pragma unroll 1
    for (int i = tid; i < 1280; i += 512) {
      const int row = i / 10, c5 = i - row * 10;
      const int c = c5 < 5 ? c5 : c5 + 5;
      u32x4* p = (u32x4*)(ep + row * ERS + c * 16);
      u32x4 v = *p;
      v[0] = phi_pk(v[0]); v[1] = phi_pk(v[1]); v[2] = phi_pk(v[2]); v[3] = phi_pk(v[3]);
      *p = v;
    }
    __syncthreads();
    STAMPA(2);
    const int hh = wave >> 1, dt = wave & 1;
    const int kcol = (hh >> 1) * 80 + (hh & 1) * 20;
    const char* abase = ep + (8 * g + q4) * ERS + (kcol + 16 * dt + 4 * p4) * 2;
    const char* bbase = ep + (8 * g + q4) * ERS + (kcol + 40 + 4 * p4) * 2;
    f32x4 c0 = f32x4{0.f, 0.f, 0.f, 0.f}, c1 = f32x4{0.f, 0.f, 0.f, 0.f};
    const bf16x8 ones = bf16x8{0x3f80, 0x3f80, 0x3f80, 0x3f80, 0x3f80, 0x3f80, 0x3f80, 0x3f80};
#pragma unroll 1
    for (int ks = 0; ks < 4; ++ks) {
      const int o = ks * 32 * ERS;
      s16x4 a0 = tr_read(abase + o), a1 = tr_read(abase + o + 4 * ERS);
      s16x4 b0 = tr_read(bbase + o), b1 = tr_read(bbase + o + 4 * ERS);
      s16x4 b2 = tr_read(bbase + o + 32), b3 = tr_read(bbase + o + 32 + 4 * ERS);
      bf16x8 a = __builtin_shufflevector(a0, a1, 0, 1, 2, 3, 4, 5, 6, 7);
      bf16x8 bA = __builtin_shufflevector(b0, b1, 0, 1, 2, 3, 4, 5, 6, 7);
      bf16x8 bB = __builtin_shufflevector(b2, b3, 0, 1, 2, 3, 4, 5, 6, 7);
      if (fr == 4) bB = ones;
      c0 = MFMA16(a, bA, c0);
      c1 = MFMA16(a, bB, c1);
    }
    float* pb = part + (size_t)(rt * NH + cg * 4 + hh) * 420;
#pragma unroll
    for (int r = 0; r < 4; ++r) {
      const int d = 16 * dt + 4 * g + r;
      if (d < 20) {
        pb[d * 20 + fr] = c0[r];
        if (fr < 4) pb[d * 20 + 16 + fr] = c1[r];
        if (fr == 4) pb[400 + d] = c1[r];
      }
    }
  }
}

__global__ __launch_bounds__(256) void k2_red(const float* __restrict__ part, u16* __restrict__ Tf) {
  __shared__ float red[420];
  const int tid = threadIdx.x, h = blockIdx.x;
#pragma unroll
  for (int rep = 0; rep < 2; ++rep) {
    const int idx = rep * 256 + tid;
    if (idx < 420) {
      float s = 0.f;
#pragma unroll
      for (int p = 0; p < NPART; ++p) s += part[(size_t)(p * NH + h) * 420 + idx];
      red[idx] = s;
    }
  }
  __syncthreads();
  if (tid < 96) {
    const int tile = tid >= 48, lane = tid - 48 * tile, fr = lane & 15, g = lane >> 4;
    const int c = fr >> 2, r = fr & 3;
    bf16x8 v;
#pragma unroll
    for (int j = 0; j < 8; ++j) {
      const int d = 8 * g + j;
      float val = 0.f;
      if (d < 20) {
        if (tile == 0) val = red[d * 20 + 5 * c + r];
        else if (r == 0) val = red[d * 20 + 5 * c + 4];
        else if (r == 1) val = red[400 + d];
      }
      v[j] = (short)f2bf(val);
    }
    *(bf16x8*)(Tf + (size_t)(h * 96 + tid) * 8) = v;
  }
}

constexpr int K3_WB = 640 * 80 * 2;
constexpr int K3_TB = 32 * 1536;
__global__ __launch_bounds__(512, 2) void k3_out(const u16* __restrict__ pq, const u16* __restrict__ Tf,
                                                 const u16* __restrict__ wo, u16* __restrict__ out) {
  __shared__ __attribute__((aligned(16))) char smem[K3_WB + K3_TB];
  const int tid = threadIdx.x, lane = tid & 63, fr = lane & 15, g = lane >> 4;
  const int wave = __builtin_amdgcn_readfirstlane(tid >> 6);
  const int rtile = wave & 3, hk = wave >> 2;
  const int bid = blockIdx.x, xcd = bid & 7, loc = bid >> 3;
  const int rt = xcd * 4 + (loc & 3), ct = loc >> 2;
  const int r0 = rt * 64, n0 = ct * 80;
  STAMPB(0);
  const u16* psrc = pq + (size_t)(r0 + rtile * 16 + fr) * D_ + hk * 320 + 8 * (g < 3 ? g : 0);
  u32x4 pb[16];
#pragma unroll
  for (int h = 0; h < 16; ++h) {
    u32x2 lo = *(const u32x2*)(psrc + h * 20), hi = *(const u32x2*)(psrc + h * 20 + 4);
    pb[h] = u32x4{lo[0], lo[1], hi[0], hi[1]};
  }
#pragma unroll
  for (int it = 0; it < 6; ++it)
    glds16(Tf + (size_t)(it * 512 + tid) * 8, smem + K3_WB + (it * 512 + wave * 64) * 16);
#pragma unroll
  for (int it = 0; it < 13; ++it) {
    int i = it * 512 + tid;
    if (it < 12 || wave < 4) {
      int half = i & 1, rp = (i >> 1) & 7, tile = i >> 4;
      int kb = tile / 5, nt = tile - kb * 5;
      int jj = rp ^ (4 * (kb & 1));
      int ks = kb >> 2, gg = kb & 3;
      int v = ks * 8 + jj;
      int head = v / 5, ii = v - head * 5;
      int krow = head * 20 + 5 * gg + ii;
      glds16(wo + (size_t)krow * D_ + n0 + nt * 16 + half * 8, smem + (it * 512 + wave * 64) * 16);
    }
  }
  STAMPB(1);
  if (wave < 4) WAITV(13); else WAITV(12);
  __builtin_amdgcn_s_barrier();
  const char* tl = smem + K3_WB + hk * 16 * 1536 + (g < 3 ? lane : fr) * 16;
  unsigned ow[40];
#pragma unroll
  for (int h = 0; h < 16; ++h) {
    u32x4 b = pb[h];
    if (g == 2) { b[2] = 0u; b[3] = 0u; }
    if (g == 3) b = u32x4{0u, 0u, 0u, 0u};
    const bf16x8 t0 = *(const bf16x8*)(tl + h * 1536);
    const bf16x8 t1 = *(const bf16x8*)(tl + h * 1536 + 768);
    const bf16x8 bb = __builtin_bit_cast(bf16x8, b);
    f32x4 z4 = f32x4{0.f, 0.f, 0.f, 0.f};
    f32x4 n0v = MFMA16(t0, bb, z4);
    f32x4 n1v = MFMA16(t1, bb, z4);
    const unsigned dn = pk2(n1v[0], n1v[1]);
    const float den = rbf(hi16(dn) + 1e-6f);
    const float rd = __builtin_amdgcn_rcpf(den);
    const unsigned p01 = pk2(n0v[0], n0v[1]), p23 = pk2(n0v[2], n0v[3]);
    const unsigned o01 = pk2(lo16(p01) * rd, hi16(p01) * rd);
    const unsigned o23 = pk2(lo16(p23) * rd, hi16(p23) * rd);
    const unsigned o4 = f2bf(lo16(dn) * rd);
    if ((h & 1) == 0) {
      ow[h * 5 / 2] = o01; ow[h * 5 / 2 + 1] = o23; ow[h * 5 / 2 + 2] = o4;
    } else {
      const int base = (h * 5) >> 1;
      ow[base] |= o01 << 16;
      ow[base + 1] = (o01 >> 16) | (o23 << 16);
      ow[base + 2] = (o23 >> 16) | (o4 << 16);
    }
  }
  STAMPB(2);
  WAITV(0);
  __builtin_amdgcn_s_barrier();
  STAMPB(3);
  const int q4 = fr >> 2, p4 = fr & 3;
  const char* b0p = smem + (hk * 40 + g) * 5 * 256 + ((q4 ^ (4 * (g & 1))) * 32) + 8 * p4;
  const char* b1p = smem + (hk * 40 + g) * 5 * 256 + (((4 + q4) ^ (4 * (g & 1))) * 32) + 8 * p4;
  f32x4 acc[5];
#pragma unroll
  for (int n = 0; n < 5; ++n) acc[n] = f32x4{0.f, 0.f, 0.f, 0.f};
  bf16x8 bc[5], bn[5];
#pragma unroll
  for (int n = 0; n < 5; ++n) {
    s16x4 lo = tr_read(b0p + n * 256), hi = tr_read(b1p + n * 256);
    bc[n] = __builtin_shufflevector(lo, hi, 0, 1, 2, 3, 4, 5, 6, 7);
  }
#pragma unroll
  for (int ks = 0; ks < 10; ++ks) {
    if (ks + 1 < 10) {
#pragma unroll
      for (int n = 0; n < 5; ++n) {
        s16x4 lo = tr_read(b0p + (ks + 1) * 20 * 256 + n * 256), hi = tr_read(b1p + (ks + 1) * 20 * 256 + n * 256);
        bn[n] = __builtin_shufflevector(lo, hi, 0, 1, 2, 3, 4, 5, 6, 7);
      }
    }
    const bf16x8 a = __builtin_bit_cast(bf16x8, (u32x4{ow[ks * 4], ow[ks * 4 + 1], ow[ks * 4 + 2], ow[ks * 4 + 3]}));
#pragma unroll
    for (int n = 0; n < 5; ++n) acc[n] = MFMA16(bc[n], a, acc[n]);
#pragma unroll
    for (int n = 0; n < 5; ++n) bc[n] = bn[n];
  }
  STAMPB(4);
  f32x4* redb = (f32x4*)(smem + K3_WB);
  if (hk == 1) {
#pragma unroll
    for (int n = 0; n < 5; ++n) redb[(rtile * 5 + n) * 64 + lane] = acc[n];
  }
  __syncthreads();
  if (hk == 0) {
    u16* dst = out + (size_t)(r0 + rtile * 16 + fr) * D_ + n0 + 4 * g;
#pragma unroll
    for (int n = 0; n < 5; ++n) {
      f32x4 s = acc[n] + redb[(rtile * 5 + n) * 64 + lane];
      *(u32x2*)(dst + 16 * n) = u32x2{pk2(s[0], s[1]), pk2(s[2], s[3])};
    }
  }
}

extern "C" void kernel_launch(void* const* d_in, const int* in_sizes, int n_in,
                              void* d_out, int out_size, void* d_ws, size_t ws_size,
                              hipStream_t stream) {
  const u16* x = (const u16*)d_in[0];
  const u16* w = (const u16*)d_in[1];
  const u16* wo = (const u16*)d_in[2];
  char* ws = (char*)d_ws;
  u16* pq = (u16*)ws;
  float* part = (float*)(ws + 2621440);
  u16* Tf = (u16*)(ws + 2621440 + 860160);
  k1_qkv<<<192, 512, 0, stream>>>(x, w, pq, part);
  k2_red<<<32, 256, 0, stream>>>(part, Tf);
  k3_out<<<256, 512, 0, stream>>>(pq, Tf, wo, (u16*)d_out);
}
```

```cpp
#include <hip/hip_runtime.h>
#include <hip/hip_bf16.h>
#include <stdint.h>

typedef __attribute__((ext_vector_type(8))) short bf16x8;
typedef __attribute__((ext_vector_type(4))) short s16x4;
typedef __attribute__((ext_vector_type(4))) float f32x4;
typedef __attribute__((ext_vector_type(4))) unsigned u32x4;
typedef __attribute__((ext_vector_type(2))) unsigned u32x2;
typedef __attribute__((address_space(3))) s16x4 lds_s16x4;
typedef unsigned short u16;

#define DI __device__ __forceinline__
#define MFMA16(a, b, c) __builtin_amdgcn_mfma_f32_16x16x32_bf16((a), (b), (c), 0, 0, 0)
#define STAMPA(i)
#define STAMPB(i)

constexpr int S_ = 2048, D_ = 640, N3 = 1920, NH = 32, DH = 20;

DI u16 f2bf(float f) { __bf16 b = (__bf16)f; return __builtin_bit_cast(u16, b); }
DI float bf2f(u16 u) { return __uint_as_float(((unsigned)u) << 16); }
DI float rbf(float f) { return bf2f(f2bf(f)); }
DI unsigned pk2(float lo, float hi) { return (unsigned)f2bf(lo) | ((unsigned)f2bf(hi) << 16); }
DI float lo16(unsigned u) { return __uint_as_float(u << 16); }
DI float hi16(unsigned u) { return __uint_as_float(u & 0xffff0000u); }
DI float phi_t(float t) {
  float a = t > 0.f ? (t + 1.0f) : __expf(t);
  return rbf(a) + 1e-6f;
}
DI unsigned phi_pk(unsigned u) { return pk2(phi_t(lo16(u)), phi_t(hi16(u))); }
DI s16x4 tr_read(const char* p) {
  return __builtin_amdgcn_ds_read_tr16_b64_v4i16((lds_s16x4*)(p));
}
DI void glds16(const void* g, char* l) {
  __builtin_amdgcn_global_load_lds((const unsigned*)g, (unsigned*)l, 16, 0, 0);
}
#define WAITV(n) asm volatile("s_waitcnt vmcnt(" #n ")" ::: "memory")

constexpr int K1_AB = 128 * 64 * 2;
constexpr int K1_BB = 64 * 160 * 2;
constexpr int K1_ST = K1_AB + K1_BB;
constexpr int K1_NBUF = 4;
constexpr int K1_NSTEP = D_ / 64;
constexpr int NPART = 16;
constexpr int ERS = 352;

__global__ __launch_bounds__(640) void k1_qkv(const u16* __restrict__ x, const u16* __restrict__ w,
                                              u16* __restrict__ pq, float* __restrict__ part) {
  __shared__ __attribute__((aligned(16))) char smem[K1_NBUF * K1_ST];
  const int bid = blockIdx.x, xcd = bid & 7, loc = bid >> 3;
  const int rt = (xcd & 3) * 4 + (loc & 3);
  const int cgi = (xcd >> 2) * 6 + (loc >> 2);
  const bool isq = cgi >= 8;
  const int cg = isq ? cgi - 8 : cgi;

  const int tid = threadIdx.x, lane = tid & 63;
  const int wave = __builtin_amdgcn_readfirstlane(tid >> 6);
  const int wr = (wave >> 1) & 3, wc = wave & 1, fr = lane & 15, g = lane >> 4;
  const int r0 = rt * 128;

  STAMPA(0);
  if (wave == 8) {
    const u16* gp[16];
#pragma unroll
    for (int it = 0; it < 16; ++it) {
      int i = it * 64 + lane;
      int row = i >> 3;
      int c = (i & 7) ^ (row & 7);
      gp[it] = x + (size_t)(r0 + row) * D_ + c * 8;
    }
    auto stage = [&](int t) {
      char* base = smem + (t & (K1_NBUF - 1)) * K1_ST;
#pragma unroll
      for (int it = 0; it < 16; ++it) glds16(gp[it] + t * 64, base + it * 1024);
    };
    stage(0); stage(1); stage(2);
#pragma unroll 1
    for (int t = 0; t < K1_NSTEP - 2; ++t) {
      WAITV(32);
      __builtin_amdgcn_s_barrier();
      if (t + 3 < K1_NSTEP) stage(t + 3); else { WAITV(16); }
    }
    __builtin_amdgcn_s_barrier();
    WAITV(0);
    __builtin_amdgcn_s_barrier();
  } else if (wave == 9) {
    const u16* gp[20];
#pragma unroll
    for (int it = 0; it < 20; ++it) {
      int i = it * 64 + lane;
      int half = i & 1, rp = (i >> 1) & 7, tile = i >> 4;
      int kb = tile / 10, nt = tile - kb * 10;
      int krow = kb * 8 + (rp ^ (4 * (kb & 1)));
      int c = 2 * nt + half;
      int seg = c / 5, wi = c - seg * 5;
      int col = isq ? (160 * cg + 8 * c) : (((seg & 1) ? 1280 : 640) + 80 * cg + (seg >> 1) * 40 + 8 * wi);
      gp[it] = w + (size_t)krow * N3 + col;
    }
    auto stage = [&](int t) {
      char* base = smem + (t & (K1_NBUF - 1)) * K1_ST + K1_AB;
#pragma unroll
      for (int it = 0; it < 20; ++it) glds16(gp[it] + (size_t)t * 64 * N3, base + it * 1024);
    };
    stage(0); stage(1); stage(2);
#pragma unroll 1
    for (int t = 0; t < K1_NSTEP - 2; ++t) {
      WAITV(40);
      __builtin_amdgcn_s_barrier();
      if (t + 3 < K1_NSTEP) stage(t + 3); else { WAITV(20); }
    }
    __builtin_amdgcn_s_barrier();
    WAITV(0);
    __builtin_amdgcn_s_barrier();
  }
  f32x4 acc[2][5];
#pragma unroll
  for (int m = 0; m < 2; ++m)
#pragma unroll
    for (int n = 0; n < 5; ++n) acc[m][n] = f32x4{0.f, 0.f, 0.f, 0.f};
  const int q4 = fr >> 2, p4 = fr & 3;
  if (wave < 8) {
    const int aoff0 = (wr * 32 + fr) * 128 + ((g ^ (fr & 7)) * 16);
    const int aoff1 = (wr * 32 + fr) * 128 + (((4 + g) ^ (fr & 7)) * 16);
    const int boff0 = K1_AB + (g * 10 + wc * 5) * 256 + ((q4 ^ (4 * (g & 1))) * 32) + 8 * p4;
    const int boff1 = K1_AB + (g * 10 + wc * 5) * 256 + (((4 + q4) ^ (4 * (g & 1))) * 32) + 8 * p4;
#pragma unroll 2
    for (int t = 0; t < K1_NSTEP; ++t) {
      __builtin_amdgcn_s_barrier();
      const char* buf = smem + (t & (K1_NBUF - 1)) * K1_ST;
#pragma unroll
      for (int kk = 0; kk < 2; ++kk) {
        bf16x8 a[2], b[5];
#pragma unroll
        for (int m = 0; m < 2; ++m) a[m] = *(const bf16x8*)(buf + (kk ? aoff1 : aoff0) + m * 2048);
#pragma unroll
        for (int n = 0; n < 5; ++n) {
          s16x4 lo = tr_read(buf + boff0 + kk * 40 * 256 + n * 256);
          s16x4 hi = tr_read(buf + boff1 + kk * 40 * 256 + n * 256);
          b[n] = __builtin_shufflevector(lo, hi, 0, 1, 2, 3, 4, 5, 6, 7);
        }
#pragma unroll
        for (int m = 0; m < 2; ++m)
#pragma unroll
          for (int n = 0; n < 5; ++n) acc[m][n] = MFMA16(b[n], a[m], acc[m][n]);
      }
    }
  }
  STAMPA(4);
  char* ep = smem + 2 * K1_ST;
  if (wave < 8)
#pragma unroll
  for (int m = 0; m < 2; ++m) {
    char* dst = ep + (wr * 32 + m * 16 + fr) * ERS + (wc * 80 + 4 * g) * 2;
#pragma unroll
    for (int n = 0; n < 5; ++n)
      *(u32x2*)(dst + 32 * n) = u32x2{pk2(acc[m][n][0], acc[m][n][1]), pk2(acc[m][n][2], acc[m][n][3])};
  }
  __syncthreads();
  STAMPA(1);
  if (isq) {
#pragma unroll 1
    for (int i = tid; i < 2560; i += 640) {
      const int row = i / 20, c = i - row * 20;
      u32x4 v = *(const u32x4*)(ep + row * ERS + c * 16);
      v[0] = phi_pk(v[0]); v[1] = phi_pk(v[1]); v[2] = phi_pk(v[2]); v[3] = phi_pk(v[3]);
      *(u32x4*)(pq + (size_t)(r0 + row) * D_ + 160 * cg + c * 8) = v;
    }
  } else {
#pragma unroll 1
    for (int i = tid; i < 1280; i += 640) {
      const int row = i / 10, c5 = i - row * 10;
      const int c = c5 < 5 ? c5 : c5 + 5;
      u32x4* p = (u32x4*)(ep + row * ERS + c * 16);
      u32x4 v = *p;
      v[0] = phi_pk(v[0]); v[1] = phi_pk(v[1]); v[2] = phi_pk(v[2]); v[3] = phi_pk(v[3]);
      *p = v;
    }
    __syncthreads();
    STAMPA(2);
    if (wave >= 8) return;
    const int hh = wave >> 1, dt = wave & 1;
    const int kcol = (hh >> 1) * 80 + (hh & 1) * 20;
    const char* abase = ep + (8 * g + q4) * ERS + (kcol + 16 * dt + 4 * p4) * 2;
    const char* bbase = ep + (8 * g + q4) * ERS + (kcol + 40 + 4 * p4) * 2;
    f32x4 c0 = f32x4{0.f, 0.f, 0.f, 0.f}, c1 = f32x4{0.f, 0.f, 0.f, 0.f};
    const bf16x8 ones = bf16x8{0x3f80, 0x3f80, 0x3f80, 0x3f80, 0x3f80, 0x3f80, 0x3f80, 0x3f80};
#pragma unroll 1
    for (int ks = 0; ks < 4; ++ks) {
      const int o = ks * 32 * ERS;
      s16x4 a0 = tr_read(abase + o), a1 = tr_read(abase + o + 4 * ERS);
      s16x4 b0 = tr_read(bbase + o), b1 = tr_read(bbase + o + 4 * ERS);
      s16x4 b2 = tr_read(bbase + o + 32), b3 = tr_read(bbase + o + 32 + 4 * ERS);
      bf16x8 a = __builtin_shufflevector(a0, a1, 0, 1, 2, 3, 4, 5, 6, 7);
      bf16x8 bA = __builtin_shufflevector(b0, b1, 0, 1, 2, 3, 4, 5, 6, 7);
      bf16x8 bB = __builtin_shufflevector(b2, b3, 0, 1, 2, 3, 4, 5, 6, 7);
      if (fr == 4) bB = ones;
      c0 = MFMA16(a, bA, c0);
      c1 = MFMA16(a, bB, c1);
    }
    float* pb = part + (size_t)(rt * NH + cg * 4 + hh) * 420;
#pragma unroll
    for (int r = 0; r < 4; ++r) {
      const int d = 16 * dt + 4 * g + r;
      if (d < 20) {
        pb[d * 20 + fr] = c0[r];
        if (fr < 4) pb[d * 20 + 16 + fr] = c1[r];
        if (fr == 4) pb[400 + d] = c1[r];
      }
    }
  }
}

__global__ __launch_bounds__(256) void k2_red(const float* __restrict__ part, u16* __restrict__ Tf) {
  __shared__ float red[420];
  const int tid = threadIdx.x, h = blockIdx.x;
#pragma unroll
  for (int rep = 0; rep < 2; ++rep) {
    const int idx = rep * 256 + tid;
    if (idx < 420) {
      float s = 0.f;
#pragma unroll
      for (int p = 0; p < NPART; ++p) s += part[(size_t)(p * NH + h) * 420 + idx];
      red[idx] = s;
    }
  }
  __syncthreads();
  if (tid < 96) {
    const int tile = tid >= 48, lane = tid - 48 * tile, fr = lane & 15, g = lane >> 4;
    const int c = fr >> 2, r = fr & 3;
    bf16x8 v;
#pragma unroll
    for (int j = 0; j < 8; ++j) {
      const int d = 8 * g + j;
      float val = 0.f;
      if (d < 20) {
        if (tile == 0) val = red[d * 20 + 5 * c + r];
        else if (r == 0) val = red[d * 20 + 5 * c + 4];
        else if (r == 1) val = red[400 + d];
      }
      v[j] = (short)f2bf(val);
    }
    *(bf16x8*)(Tf + (size_t)(h * 96 + tid) * 8) = v;
  }
}

constexpr int K3_WB = 640 * 80 * 2;
constexpr int K3_TB = 32 * 1536;
typedef __attribute__((ext_vector_type(4), aligned(8))) unsigned u32x4a8;
__global__ __launch_bounds__(640) void k3_out(const u16* __restrict__ pq, const u16* __restrict__ Tf,
                                              const u16* __restrict__ wo, u16* __restrict__ out) {
  __shared__ __attribute__((aligned(16))) char smem[K3_WB + K3_TB];
  const int tid = threadIdx.x, lane = tid & 63, fr = lane & 15, g = lane >> 4;
  const int wave = __builtin_amdgcn_readfirstlane(tid >> 6);
  const int rp = wave & 1, kq = (wave >> 1) & 3;
  const int bid = blockIdx.x, xcd = bid & 7, loc = bid >> 3;
  const int rt = xcd * 4 + (loc & 3), ct = loc >> 2;
  const int r0 = rt * 64, n0 = ct * 80;
  STAMPB(0);
  f32x4 acc[2][5];
#pragma unroll
  for (int m = 0; m < 2; ++m)
#pragma unroll
    for (int n = 0; n < 5; ++n) acc[m][n] = f32x4{0.f, 0.f, 0.f, 0.f};
  if (wave >= 8) {
    const int lw = wave - 8;
#pragma unroll 4
    for (int it = 0; it < 24; ++it) {
      const int pc = lw * 24 + it;
      glds16(Tf + (size_t)(pc * 64 + lane) * 8, smem + K3_WB + pc * 1024);
    }
#pragma unroll 2
    for (int it = 0; it < 50; ++it) {
      const int pc = it * 2 + lw;
      int i = pc * 64 + lane;
      int half = i & 1, rpp = (i >> 1) & 7, tile = i >> 4;
      int kb = tile / 5, nt = tile - kb * 5;
      int jj = rpp ^ (4 * (kb & 1));
      int ks = kb >> 2, gg = kb & 3;
      int v = ks * 8 + jj;
      int head = v / 5, ii = v - head * 5;
      int krow = head * 20 + 5 * gg + ii;
      glds16(wo + (size_t)krow * D_ + n0 + nt * 16 + half * 8, smem + pc * 1024);
    }
    WAITV(50);
    __builtin_amdgcn_s_barrier();
    WAITV(0);
    __builtin_amdgcn_s_barrier();
  } else {
    const u16* psrc = pq + (size_t)(r0 + rp * 32 + fr) * D_ + kq * 160 + 8 * (g < 3 ? g : 0);
    u32x4 pb[2][8];
#pragma unroll
    for (int m = 0; m < 2; ++m)
#pragma unroll
      for (int h = 0; h < 8; ++h) pb[m][h] = *(const u32x4a8*)(psrc + m * 16 * D_ + h * 20);
    STAMPB(1);
    __builtin_amdgcn_s_barrier();
    const char* tl = smem + K3_WB + kq * 8 * 1536 + (g < 3 ? lane : fr) * 16;
    unsigned ow[2][20];
#pragma unroll
    for (int h = 0; h < 8; ++h) {
      const bf16x8 t0 = *(const bf16x8*)(tl + h * 1536);
      const bf16x8 t1 = *(const bf16x8*)(tl + h * 1536 + 768);
#pragma unroll
      for (int m = 0; m < 2; ++m) {
        u32x4 b = pb[m][h];
        if (g == 2) { b[2] = 0u; b[3] = 0u; }
        if (g == 3) b = u32x4{0u, 0u, 0u, 0u};
        const bf16x8 bb = __builtin_bit_cast(bf16x8, b);
        f32x4 z4 = f32x4{0.f, 0.f, 0.f, 0.f};
        f32x4 n0v = MFMA16(t0, bb, z4);
        f32x4 n1v = MFMA16(t1, bb, z4);
        const unsigned dn = pk2(n1v[0], n1v[1]);
        const float den = rbf(hi16(dn) + 1e-6f);
        const float rd = __builtin_amdgcn_rcpf(den);
        const unsigned p01 = pk2(n0v[0], n0v[1]), p23 = pk2(n0v[2], n0v[3]);
        const unsigned o01 = pk2(lo16(p01) * rd, hi16(p01) * rd);
        const unsigned o23 = pk2(lo16(p23) * rd, hi16(p23) * rd);
        const unsigned o4 = f2bf(lo16(dn) * rd);
        if ((h & 1) == 0) {
          ow[m][h * 5 / 2] = o01; ow[m][h * 5 / 2 + 1] = o23; ow[m][h * 5 / 2 + 2] = o4;
        } else {
          const int base = (h * 5) >> 1;
          ow[m][base] |= o01 << 16;
          ow[m][base + 1] = (o01 >> 16) | (o23 << 16);
          ow[m][base + 2] = (o23 >> 16) | (o4 << 16);
        }
      }
    }
    STAMPB(2);
    __builtin_amdgcn_s_barrier();
    STAMPB(3);
    const int q4 = fr >> 2, p4 = fr & 3;
    const char* b0p = smem + (kq * 20 + g) * 5 * 256 + ((q4 ^ (4 * (g & 1))) * 32) + 8 * p4;
    const char* b1p = smem + (kq * 20 + g) * 5 * 256 + (((4 + q4) ^ (4 * (g & 1))) * 32) + 8 * p4;
#pragma unroll
    for (int ks = 0; ks < 5; ++ks) {
      bf16x8 bc[5];
#pragma unroll
      for (int n = 0; n < 5; ++n) {
        s16x4 lo = tr_read(b0p + ks * 20 * 256 + n * 256), hi = tr_read(b1p + ks * 20 * 256 + n * 256);
        bc[n] = __builtin_shufflevector(lo, hi, 0, 1, 2, 3, 4, 5, 6, 7);
      }
#pragma unroll
      for (int m = 0; m < 2; ++m) {
        const bf16x8 a = __builtin_bit_cast(bf16x8, (u32x4{ow[m][ks * 4], ow[m][ks * 4 + 1], ow[m][ks * 4 + 2], ow[m][ks * 4 + 3]}));
#pragma unroll
        for (int n = 0; n < 5; ++n) acc[m][n] = MFMA16(bc[n], a, acc[m][n]);
      }
    }
    STAMPB(4);
  }
  __syncthreads();
  f32x4* redb = (f32x4*)smem;
  if (wave < 8) {
#pragma unroll
    for (int m = 0; m < 2; ++m)
#pragma unroll
      for (int n = 0; n < 5; ++n) redb[(kq * 20 + (rp * 2 + m) * 5 + n) * 64 + lane] = acc[m][n];
  }
  __syncthreads();
#pragma unroll
  for (int it = 0; it < 2; ++it) {
    const int i = it * 640 + tid;
    const int tile = i >> 6, l2 = i & 63;
    const int rtl = tile / 5, n = tile - rtl * 5;
    f32x4 sum = redb[i] + redb[1280 + i] + redb[2560 + i] + redb[3840 + i];
    u16* dst = out + (size_t)(r0 + rtl * 16 + (l2 & 15)) * D_ + n0 + 16 * n + 4 * (l2 >> 4);
    *(u32x2*)dst = u32x2{pk2(sum[0], sum[1]), pk2(sum[2], sum[3])};
  }
}

extern "C" void kernel_launch(void* const* d_in, const int* in_sizes, int n_in,
                              void* d_out, int out_size, void* d_ws, size_t ws_size,
                              hipStream_t stream) {
  const u16* x = (const u16*)d_in[0];
  const u16* w = (const u16*)d_in[1];
  const u16* wo = (const u16*)d_in[2];
  char* ws = (char*)d_ws;
  u16* pq = (u16*)ws;
  float* part = (float*)(ws + 2621440);
  u16* Tf = (u16*)(ws + 2621440 + 860160);
  k1_qkv<<<192, 640, 0, stream>>>(x, w, pq, part);
  k2_red<<<32, 256, 0, stream>>>(part, Tf);
  k3_out<<<256, 640, 0, stream>>>(pq, Tf, wo, (u16*)d_out);
}
```

```cpp
#include <hip/hip_runtime.h>
#include <hip/hip_bf16.h>
#include <stdint.h>

typedef __attribute__((ext_vector_type(8))) short bf16x8;
typedef __attribute__((ext_vector_type(4))) short s16x4;
typedef __attribute__((ext_vector_type(4))) float f32x4;
typedef __attribute__((ext_vector_type(4))) unsigned u32x4;
typedef __attribute__((ext_vector_type(2))) unsigned u32x2;
typedef __attribute__((address_space(3))) s16x4 lds_s16x4;
typedef unsigned short u16;

#define DI __device__ __forceinline__
#define MFMA16(a, b, c) __builtin_amdgcn_mfma_f32_16x16x32_bf16((a), (b), (c), 0, 0, 0)
#define STAMPA(i)
#define STAMPB(i)

constexpr int S_ = 2048, D_ = 640, N3 = 1920, NH = 32, DH = 20;

DI u16 f2bf(float f) { __bf16 b = (__bf16)f; return __builtin_bit_cast(u16, b); }
DI float bf2f(u16 u) { return __uint_as_float(((unsigned)u) << 16); }
DI float rbf(float f) { return bf2f(f2bf(f)); }
DI unsigned pk2(float lo, float hi) { return (unsigned)f2bf(lo) | ((unsigned)f2bf(hi) << 16); }
DI float lo16(unsigned u) { return __uint_as_float(u << 16); }
DI float hi16(unsigned u) { return __uint_as_float(u & 0xffff0000u); }
DI float phi_t(float t) {
  float a = t > 0.f ? (t + 1.0f) : __expf(t);
  return rbf(a) + 1e-6f;
}
DI unsigned phi_pk(unsigned u) { return pk2(phi_t(lo16(u)), phi_t(hi16(u))); }
DI s16x4 tr_read(const char* p) {
  return __builtin_amdgcn_ds_read_tr16_b64_v4i16((lds_s16x4*)(p));
}
DI void glds16(const void* g, char* l) {
  __builtin_amdgcn_global_load_lds((const unsigned*)g, (unsigned*)l, 16, 0, 0);
}
#define WAITV(n) asm volatile("s_waitcnt vmcnt(" #n ")" ::: "memory")

constexpr int K1_AB = 128 * 64 * 2;
constexpr int K1_BB = 64 * 160 * 2;
constexpr int K1_ST = K1_AB + K1_BB;
constexpr int K1_NBUF = 4;
constexpr int K1_NSTEP = D_ / 64;
constexpr int NPART = 16;
constexpr int ERS = 352;

__global__ __launch_bounds__(640) void k1_qkv(const u16* __restrict__ x, const u16* __restrict__ w,
                                              u16* __restrict__ pq, float* __restrict__ part,
                                              unsigned* __restrict__ cnt) {
  __shared__ __attribute__((aligned(16))) char smem[K1_NBUF * K1_ST];
  if (blockIdx.x == 0 && threadIdx.x == 0) *cnt = 0u;
  const int bid = blockIdx.x, xcd = bid & 7, loc = bid >> 3;
  const int rt = (xcd & 3) * 4 + (loc & 3);
  const int cgi = (xcd >> 2) * 6 + (loc >> 2);
  const bool isq = cgi >= 8;
  const int cg = isq ? cgi - 8 : cgi;

  const int tid = threadIdx.x, lane = tid & 63;
  const int wave = __builtin_amdgcn_readfirstlane(tid >> 6);
  const int wr = (wave >> 1) & 3, wc = wave & 1, fr = lane & 15, g = lane >> 4;
  const int r0 = rt * 128;

  STAMPA(0);
  if (wave == 8) {
    const u16* gp[16];
#pragma unroll
    for (int it = 0; it < 16; ++it) {
      int i = it * 64 + lane;
      int row = i >> 3;
      int c = (i & 7) ^ (row & 7);
      gp[it] = x + (size_t)(r0 + row) * D_ + c * 8;
    }
    auto stage = [&](int t) {
      char* base = smem + (t & (K1_NBUF - 1)) * K1_ST;
#pragma unroll
      for (int it = 0; it < 16; ++it) glds16(gp[it] + t * 64, base + it * 1024);
    };
    stage(0); stage(1); stage(2);
#pragma unroll 1
    for (int t = 0; t < K1_NSTEP - 2; ++t) {
      WAITV(32);
      __builtin_amdgcn_s_barrier();
      if (t + 3 < K1_NSTEP) stage(t + 3); else { WAITV(16); }
    }
    __builtin_amdgcn_s_barrier();
    WAITV(0);
    __builtin_amdgcn_s_barrier();
  } else if (wave == 9) {
    const u16* gp[20];
#pragma unroll
    for (int it = 0; it < 20; ++it) {
      int i = it * 64 + lane;
      int half = i & 1, rp = (i >> 1) & 7, tile = i >> 4;
      int kb = tile / 10, nt = tile - kb * 10;
      int krow = kb * 8 + (rp ^ (4 * (kb & 1)));
      int c = 2 * nt + half;
      int seg = c / 5, wi = c - seg * 5;
      int col = isq ? (160 * cg + 8 * c) : (((seg & 1) ? 1280 : 640) + 80 * cg + (seg >> 1) * 40 + 8 * wi);
      gp[it] = w + (size_t)krow * N3 + col;
    }
    auto stage = [&](int t) {
      char* base = smem + (t & (K1_NBUF - 1)) * K1_ST + K1_AB;
#pragma unroll
      for (int it = 0; it < 20; ++it) glds16(gp[it] + (size_t)t * 64 * N3, base + it * 1024);
    };
    stage(0); stage(1); stage(2);
#pragma unroll 1
    for (int t = 0; t < K1_NSTEP - 2; ++t) {
      WAITV(40);
      __builtin_amdgcn_s_barrier();
      if (t + 3 < K1_NSTEP) stage(t + 3); else { WAITV(20); }
    }
    __builtin_amdgcn_s_barrier();
    WAITV(0);
    __builtin_amdgcn_s_barrier();
  }
  f32x4 acc[2][5];
#pragma unroll
  for (int m = 0; m < 2; ++m)
#pragma unroll
    for (int n = 0; n < 5; ++n) acc[m][n] = f32x4{0.f, 0.f, 0.f, 0.f};
  const int q4 = fr >> 2, p4 = fr & 3;
  if (wave < 8) {
    const int aoff0 = (wr * 32 + fr) * 128 + ((g ^ (fr & 7)) * 16);
    const int aoff1 = (wr * 32 + fr) * 128 + (((4 + g) ^ (fr & 7)) * 16);
    const int boff0 = K1_AB + (g * 10 + wc * 5) * 256 + ((q4 ^ (4 * (g & 1))) * 32) + 8 * p4;
    const int boff1 = K1_AB + (g * 10 + wc * 5) * 256 + (((4 + q4) ^ (4 * (g & 1))) * 32) + 8 * p4;
#pragma unroll 2
    for (int t = 0; t < K1_NSTEP; ++t) {
      __builtin_amdgcn_s_barrier();
      const char* buf = smem + (t & (K1_NBUF - 1)) * K1_ST;
#pragma unroll
      for (int kk = 0; kk < 2; ++kk) {
        bf16x8 a[2], b[5];
#pragma unroll
        for (int m = 0; m < 2; ++m) a[m] = *(const bf16x8*)(buf + (kk ? aoff1 : aoff0) + m * 2048);
#pragma unroll
        for (int n = 0; n < 5; ++n) {
          s16x4 lo = tr_read(buf + boff0 + kk * 40 * 256 + n * 256);
          s16x4 hi = tr_read(buf + boff1 + kk * 40 * 256 + n * 256);
          b[n] = __builtin_shufflevector(lo, hi, 0, 1, 2, 3, 4, 5, 6, 7);
        }
#pragma unroll
        for (int m = 0; m < 2; ++m)
#pragma unroll
          for (int n = 0; n < 5; ++n) acc[m][n] = MFMA16(b[n], a[m], acc[m][n]);
      }
    }
  }
  STAMPA(4);
  char* ep = smem + 2 * K1_ST;
  if (wave < 8)
#pragma unroll
  for (int m = 0; m < 2; ++m) {
    char* dst = ep + (wr * 32 + m * 16 + fr) * ERS + (wc * 80 + 4 * g) * 2;
#pragma unroll
    for (int n = 0; n < 5; ++n)
      *(u32x2*)(dst + 32 * n) = u32x2{pk2(acc[m][n][0], acc[m][n][1]), pk2(acc[m][n][2], acc[m][n][3])};
  }
  __syncthreads();
  STAMPA(1);
  if (isq) {
#pragma unroll 1
    for (int i = tid; i < 2560; i += 640) {
      const int row = i / 20, c = i - row * 20;
      u32x4 v = *(const u32x4*)(ep + row * ERS + c * 16);
      v[0] = phi_pk(v[0]); v[1] = phi_pk(v[1]); v[2] = phi_pk(v[2]); v[3] = phi_pk(v[3]);
      *(u32x4*)(pq + (size_t)(r0 + row) * D_ + 160 * cg + c * 8) = v;
    }
  } else {
#pragma unroll 1
    for (int i = tid; i < 1280; i += 640) {
      const int row = i / 10, c5 = i - row * 10;
      const int c = c5 < 5 ? c5 : c5 + 5;
      u32x4* p = (u32x4*)(ep + row * ERS + c * 16);
      u32x4 v = *p;
      v[0] = phi_pk(v[0]); v[1] = phi_pk(v[1]); v[2] = phi_pk(v[2]); v[3] = phi_pk(v[3]);
      *p = v;
    }
    __syncthreads();
    STAMPA(2);
    if (wave >= 8) return;
    const int hh = wave >> 1, dt = wave & 1;
    const int kcol = (hh >> 1) * 80 + (hh & 1) * 20;
    const char* abase = ep + (8 * g + q4) * ERS + (kcol + 16 * dt + 4 * p4) * 2;
    const char* bbase = ep + (8 * g + q4) * ERS + (kcol + 40 + 4 * p4) * 2;
    f32x4 c0 = f32x4{0.f, 0.f, 0.f, 0.f}, c1 = f32x4{0.f, 0.f, 0.f, 0.f};
    const bf16x8 ones = bf16x8{0x3f80, 0x3f80, 0x3f80, 0x3f80, 0x3f80, 0x3f80, 0x3f80, 0x3f80};
#pragma unroll 1
    for (int ks = 0; ks < 4; ++ks) {
      const int o = ks * 32 * ERS;
      s16x4 a0 = tr_read(abase + o), a1 = tr_read(abase + o + 4 * ERS);
      s16x4 b0 = tr_read(bbase + o), b1 = tr_read(bbase + o + 4 * ERS);
      s16x4 b2 = tr_read(bbase + o + 32), b3 = tr_read(bbase + o + 32 + 4 * ERS);
      bf16x8 a = __builtin_shufflevector(a0, a1, 0, 1, 2, 3, 4, 5, 6, 7);
      bf16x8 bA = __builtin_shufflevector(b0, b1, 0, 1, 2, 3, 4, 5, 6, 7);
      bf16x8 bB = __builtin_shufflevector(b2, b3, 0, 1, 2, 3, 4, 5, 6, 7);
      if (fr == 4) bB = ones;
      c0 = MFMA16(a, bA, c0);
      c1 = MFMA16(a, bB, c1);
    }
    float* pb = part + (size_t)(rt * NH + cg * 4 + hh) * 420;
#pragma unroll
    for (int r = 0; r < 4; ++r) {
      const int d = 16 * dt + 4 * g + r;
      if (d < 20) {
        pb[d * 20 + fr] = c0[r];
        if (fr < 4) pb[d * 20 + 16 + fr] = c1[r];
        if (fr == 4) pb[400 + d] = c1[r];
      }
    }
  }
}

constexpr int K3_WB = 640 * 80 * 2;
constexpr int K3_TB = 32 * 1536;
constexpr int K3_RB = 1792;
typedef __attribute__((ext_vector_type(4), aligned(8))) unsigned u32x4a8;
typedef __attribute__((address_space(1))) unsigned gu32;
typedef __attribute__((address_space(1))) unsigned long long gu64;
__global__ __launch_bounds__(640) void k3_out(const u16* __restrict__ pq, const float* __restrict__ part,
                                              u16* Tf, unsigned* cnt,
                                              const u16* __restrict__ wo, u16* __restrict__ out) {
  __shared__ __attribute__((aligned(16))) char smem[K3_WB + K3_TB + K3_RB];
  const int tid = threadIdx.x, lane = tid & 63, fr = lane & 15, g = lane >> 4;
  const int wave = __builtin_amdgcn_readfirstlane(tid >> 6);
  const int rp = wave & 1, kq = (wave >> 1) & 3;
  const int bid = blockIdx.x, xcd = bid & 7, loc = bid >> 3;
  const int rt = xcd * 4 + (loc & 3), ct = loc >> 2;
  const int r0 = rt * 64, n0 = ct * 80;
  STAMPB(0);
  f32x4 acc[2][5];
#pragma unroll
  for (int m = 0; m < 2; ++m)
#pragma unroll
    for (int n = 0; n < 5; ++n) acc[m][n] = f32x4{0.f, 0.f, 0.f, 0.f};
  if (wave >= 8) {
    const int lw = wave - 8;
    if (lw == 0 && bid < NH) {
      const f32x4* pp = (const f32x4*)(part + (size_t)bid * 420);
      f32x4 s0 = f32x4{0.f, 0.f, 0.f, 0.f}, s1 = f32x4{0.f, 0.f, 0.f, 0.f};
      const int l1 = lane < 41 ? 64 + lane : 64;
#pragma unroll
      for (int p = 0; p < NPART; ++p) {
        s0 += pp[(size_t)p * NH * 105 + lane];
        s1 += pp[(size_t)p * NH * 105 + l1];
      }
      f32x4* red4 = (f32x4*)(smem + K3_WB + K3_TB);
      red4[lane] = s0;
      if (lane < 41) red4[64 + lane] = s1;
      asm volatile("s_waitcnt lgkmcnt(0)" ::: "memory");
      const float* red = (const float*)red4;
#pragma unroll
      for (int tile = 0; tile < 2; ++tile) {
        if (lane < 48) {
          const int c = fr >> 2, r = fr & 3;
          unsigned v[4];
#pragma unroll
          for (int j2 = 0; j2 < 4; ++j2) {
            float val[2];
#pragma unroll
            for (int jj = 0; jj < 2; ++jj) {
              const int d = 8 * g + 2 * j2 + jj;
              float t = 0.f;
              if (d < 20) {
                if (tile == 0) t = red[d * 20 + 5 * c + r];
                else if (r == 0) t = red[d * 20 + 5 * c + 4];
                else if (r == 1) t = red[400 + d];
              }
              val[jj] = t;
            }
            v[j2] = pk2(val[0], val[1]);
          }
          gu64* dst = (gu64*)(Tf + (size_t)(bid * 96 + tile * 48 + lane) * 8);
          __hip_atomic_store(dst, (unsigned long long)v[0] | ((unsigned long long)v[1] << 32), __ATOMIC_RELAXED, __HIP_MEMORY_SCOPE_AGENT);
          __hip_atomic_store(dst + 1, (unsigned long long)v[2] | ((unsigned long long)v[3] << 32), __ATOMIC_RELAXED, __HIP_MEMORY_SCOPE_AGENT);
        }
      }
      WAITV(0);
      if (lane == 0) __hip_atomic_fetch_add((gu32*)cnt, 1u, __ATOMIC_RELAXED, __HIP_MEMORY_SCOPE_AGENT);
    }
    {
      unsigned spins = 0;
      while (__hip_atomic_load((gu32*)cnt, __ATOMIC_RELAXED, __HIP_MEMORY_SCOPE_AGENT) < (unsigned)NH) {
        __builtin_amdgcn_s_sleep(1);
        if (++spins > (1u << 22)) break;
      }
    }
    asm volatile("" ::: "memory");
    const auto rs = __builtin_amdgcn_make_buffer_rsrc((void*)Tf, 0, K3_TB, 0x00020000);
#pragma unroll
    for (int hb = 0; hb < 2; ++hb) {
      u32x4 tv[12];
#pragma unroll
      for (int it = 0; it < 12; ++it)
        tv[it] = __builtin_amdgcn_raw_buffer_load_b128(rs, ((lw * 24 + hb * 12 + it) * 64 + lane) * 16, 0, 16);
#pragma unroll
      for (int it = 0; it < 12; ++it)
        *(u32x4*)(smem + K3_WB + ((lw * 24 + hb * 12 + it) * 64 + lane) * 16) = tv[it];
    }
    asm volatile("s_waitcnt lgkmcnt(0)" ::: "memory");
    __builtin_amdgcn_s_barrier();
    __builtin_amdgcn_s_barrier();
  } else {
    const u16* psrc = pq + (size_t)(r0 + rp * 32 + fr) * D_ + kq * 160 + 8 * (g < 3 ? g : 0);
    u32x4 pb[2][8];
#pragma unroll
    for (int m = 0; m < 2; ++m)
#pragma unroll
      for (int h = 0; h < 8; ++h) pb[m][h] = *(const u32x4a8*)(psrc + m * 16 * D_ + h * 20);
#pragma unroll
    for (int it = 0; it < 13; ++it) {
      const int pc = it * 8 + wave;
      if (pc < 100) {
        int i = pc * 64 + lane;
        int half = i & 1, rpp = (i >> 1) & 7, tile = i >> 4;
        int kb = tile / 5, nt = tile - kb * 5;
        int jj = rpp ^ (4 * (kb & 1));
        int ks = kb >> 2, gg = kb & 3;
        int v = ks * 8 + jj;
        int head = v / 5, ii = v - head * 5;
        int krow = head * 20 + 5 * gg + ii;
        glds16(wo + (size_t)krow * D_ + n0 + nt * 16 + half * 8, smem + pc * 1024);
      }
    }
    STAMPB(1);
    __builtin_amdgcn_s_barrier();
    const char* tl = smem + K3_WB + kq * 8 * 1536 + (g < 3 ? lane : fr) * 16;
    unsigned ow[2][20];
#pragma unroll
    for (int h = 0; h < 8; ++h) {
      const bf16x8 t0 = *(const bf16x8*)(tl + h * 1536);
      const bf16x8 t1 = *(const bf16x8*)(tl + h * 1536 + 768);
#pragma unroll
      for (int m = 0; m < 2; ++m) {
        u32x4 b = pb[m][h];
        if (g == 2) { b[2] = 0u; b[3] = 0u; }
        if (g == 3) b = u32x4{0u, 0u, 0u, 0u};
        const bf16x8 bb = __builtin_bit_cast(bf16x8, b);
        f32x4 z4 = f32x4{0.f, 0.f, 0.f, 0.f};
        f32x4 n0v = MFMA16(t0, bb, z4);
        f32x4 n1v = MFMA16(t1, bb, z4);
        const unsigned dn = pk2(n1v[0], n1v[1]);
        const float den = rbf(hi16(dn) + 1e-6f);
        const float rd = __builtin_amdgcn_rcpf(den);
        const unsigned p01 = pk2(n0v[0], n0v[1]), p23 = pk2(n0v[2], n0v[3]);
        const unsigned o01 = pk2(lo16(p01) * rd, hi16(p01) * rd);
        const unsigned o23 = pk2(lo16(p23) * rd, hi16(p23) * rd);
        const unsigned o4 = f2bf(lo16(dn) * rd);
        if ((h & 1) == 0) {
          ow[m][h * 5 / 2] = o01; ow[m][h * 5 / 2 + 1] = o23; ow[m][h * 5 / 2 + 2] = o4;
        } else {
          const int base = (h * 5) >> 1;
          ow[m][base] |= o01 << 16;
          ow[m][base + 1] = (o01 >> 16) | (o23 << 16);
          ow[m][base + 2] = (o23 >> 16) | (o4 << 16);
        }
      }
    }
    STAMPB(2);
    WAITV(0);
    __builtin_amdgcn_s_barrier();
    STAMPB(3);
    const int q4 = fr >> 2, p4 = fr & 3;
    const char* b0p = smem + (kq * 20 + g) * 5 * 256 + ((q4 ^ (4 * (g & 1))) * 32) + 8 * p4;
    const char* b1p = smem + (kq * 20 + g) * 5 * 256 + (((4 + q4) ^ (4 * (g & 1))) * 32) + 8 * p4;
#pragma unroll
    for (int ks = 0; ks < 5; ++ks) {
      bf16x8 bc[5];
#pragma unroll
      for (int n = 0; n < 5; ++n) {
        s16x4 lo = tr_read(b0p + ks * 20 * 256 + n * 256), hi = tr_read(b1p + ks * 20 * 256 + n * 256);
        bc[n] = __builtin_shufflevector(lo, hi, 0, 1, 2, 3, 4, 5, 6, 7);
      }
#pragma unroll
      for (int m = 0; m < 2; ++m) {
        const bf16x8 a = __builtin_bit_cast(bf16x8, (u32x4{ow[m][ks * 4], ow[m][ks * 4 + 1], ow[m][ks * 4 + 2], ow[m][ks * 4 + 3]}));
#pragma unroll
        for (int n = 0; n < 5; ++n) acc[m][n] = MFMA16(bc[n], a, acc[m][n]);
      }
    }
    STAMPB(4);
  }
  __syncthreads();
  f32x4* redb = (f32x4*)smem;
  if (wave < 8) {
#pragma unroll
    for (int m = 0; m < 2; ++m)
#pragma unroll
      for (int n = 0; n < 5; ++n) redb[(kq * 20 + (rp * 2 + m) * 5 + n) * 64 + lane] = acc[m][n];
  }
  __syncthreads();
#pragma unroll
  for (int it = 0; it < 2; ++it) {
    const int i = it * 640 + tid;
    const int tile = i >> 6, l2 = i & 63;
    const int rtl = tile / 5, n = tile - rtl * 5;
    f32x4 sum = redb[i] + redb[1280 + i] + redb[2560 + i] + redb[3840 + i];
    u16* dst = out + (size_t)(r0 + rtl * 16 + (l2 & 15)) * D_ + n0 + 16 * n + 4 * (l2 >> 4);
    *(u32x2*)dst = u32x2{pk2(sum[0], sum[1]), pk2(sum[2], sum[3])};
  }
}

extern "C" void kernel_launch(void* const* d_in, const int* in_sizes, int n_in,
                              void* d_out, int out_size, void* d_ws, size_t ws_size,
                              hipStream_t stream) {
  const u16* x = (const u16*)d_in[0];
  const u16* w = (const u16*)d_in[1];
  const u16* wo = (const u16*)d_in[2];
  char* ws = (char*)d_ws;
  u16* pq = (u16*)ws;
  float* part = (float*)(ws + 2621440);
  u16* Tf = (u16*)(ws + 2621440 + 860160);
  unsigned* cnt = (unsigned*)(ws + 2621440 + 860160 + 65536);
  k1_qkv<<<192, 640, 0, stream>>>(x, w, pq, part, cnt);
  k3_out<<<256, 640, 0, stream>>>(pq, part, Tf, cnt, wo, (u16*)d_out);
}
```

```cpp
#include <hip/hip_runtime.h>
#include <hip/hip_bf16.h>
#include <stdint.h>

typedef __attribute__((ext_vector_type(8))) short bf16x8;
typedef __attribute__((ext_vector_type(4))) short s16x4;
typedef __attribute__((ext_vector_type(4))) float f32x4;
typedef __attribute__((ext_vector_type(4))) unsigned u32x4;
typedef __attribute__((ext_vector_type(2))) unsigned u32x2;
typedef __attribute__((address_space(3))) s16x4 lds_s16x4;
typedef unsigned short u16;

#define DI __device__ __forceinline__
#define MFMA16(a, b, c) __builtin_amdgcn_mfma_f32_16x16x32_bf16((a), (b), (c), 0, 0, 0)
#define STAMPA(i)
#define STAMPB(i)

constexpr int S_ = 2048, D_ = 640, N3 = 1920, NH = 32, DH = 20;

DI u16 f2bf(float f) { __bf16 b = (__bf16)f; return __builtin_bit_cast(u16, b); }
DI float bf2f(u16 u) { return __uint_as_float(((unsigned)u) << 16); }
DI float rbf(float f) { return bf2f(f2bf(f)); }
DI unsigned pk2(float lo, float hi) { return (unsigned)f2bf(lo) | ((unsigned)f2bf(hi) << 16); }
DI float lo16(unsigned u) { return __uint_as_float(u << 16); }
DI float hi16(unsigned u) { return __uint_as_float(u & 0xffff0000u); }
DI float phi_t(float t) {
  float a = t > 0.f ? (t + 1.0f) : __expf(t);
  return rbf(a) + 1e-6f;
}
DI unsigned phi_pk(unsigned u) { return pk2(phi_t(lo16(u)), phi_t(hi16(u))); }
DI s16x4 tr_read(const char* p) {
  return __builtin_amdgcn_ds_read_tr16_b64_v4i16((lds_s16x4*)(p));
}
DI void glds16(const void* g, char* l) {
  __builtin_amdgcn_global_load_lds((const unsigned*)g, (unsigned*)l, 16, 0, 0);
}
#define WAITV(n) asm volatile("s_waitcnt vmcnt(" #n ")" ::: "memory")

constexpr int K1_AB = 128 * 64 * 2;
constexpr int K1_BB = 64 * 160 * 2;
constexpr int K1_ST = K1_AB + K1_BB;
constexpr int K1_NBUF = 4;
constexpr int K1_NSTEP = D_ / 64;
constexpr int NPART = 16;
constexpr int ERS = 352;

__global__ __launch_bounds__(640) void k1_qkv(const u16* __restrict__ x, const u16* __restrict__ w,
                                              u16* __restrict__ pq, float* __restrict__ part) {
  __shared__ __attribute__((aligned(16))) char smem[K1_NBUF * K1_ST];
  const int bid = blockIdx.x, xcd = bid & 7, loc = bid >> 3;
  const int rt = (xcd & 3) * 4 + (loc & 3);
  const int cgi = (xcd >> 2) * 6 + (loc >> 2);
  const bool isq = cgi >= 8;
  const int cg = isq ? cgi - 8 : cgi;

  const int tid = threadIdx.x, lane = tid & 63;
  const int wave = __builtin_amdgcn_readfirstlane(tid >> 6);
  const int wr = (wave >> 1) & 3, wc = wave & 1, fr = lane & 15, g = lane >> 4;
  const int r0 = rt * 128;

  STAMPA(0);
  if (wave == 8) {
    const u16* gp[16];
#pragma unroll
    for (int it = 0; it < 16; ++it) {
      int i = it * 64 + lane;
      int row = i >> 3;
      int c = (i & 7) ^ (row & 7);
      gp[it] = x + (size_t)(r0 + row) * D_ + c * 8;
    }
    auto stage = [&](int t) {
      char* base = smem + (t & (K1_NBUF - 1)) * K1_ST;
#pragma unroll
      for (int it = 0; it < 16; ++it) glds16(gp[it] + t * 64, base + it * 1024);
    };
    stage(0); stage(1); stage(2);
#pragma unroll 1
    for (int t = 0; t < K1_NSTEP - 2; ++t) {
      WAITV(32);
      __builtin_amdgcn_s_barrier();
      if (t + 3 < K1_NSTEP) stage(t + 3); else { WAITV(16); }
    }
    __builtin_amdgcn_s_barrier();
    WAITV(0);
    __builtin_amdgcn_s_barrier();
  } else if (wave == 9) {
    const u16* gp[20];
#pragma unroll
    for (int it = 0; it < 20; ++it) {
      int i = it * 64 + lane;
      int half = i & 1, rp = (i >> 1) & 7, tile = i >> 4;
      int kb = tile / 10, nt = tile - kb * 10;
      int krow = kb * 8 + (rp ^ (4 * (kb & 1)));
      int c = 2 * nt + half;
      int seg = c / 5, wi = c - seg * 5;
      int col = isq ? (160 * cg + 8 * c) : (((seg & 1) ? 1280 : 640) + 80 * cg + (seg >> 1) * 40 + 8 * wi);
      gp[it] = w + (size_t)krow * N3 + col;
    }
    auto stage = [&](int t) {
      char* base = smem + (t & (K1_NBUF - 1)) * K1_ST + K1_AB;
#pragma unroll
      for (int it = 0; it < 20; ++it) glds16(gp[it] + (size_t)t * 64 * N3, base + it * 1024);
    };
    stage(0); stage(1); stage(2);
#pragma unroll 1
    for (int t = 0; t < K1_NSTEP - 2; ++t) {
      WAITV(40);
      __builtin_amdgcn_s_barrier();
      if (t + 3 < K1_NSTEP) stage(t + 3); else { WAITV(20); }
    }
    __builtin_amdgcn_s_barrier();
    WAITV(0);
    __builtin_amdgcn_s_barrier();
  }
  f32x4 acc[2][5];
#pragma unroll
  for (int m = 0; m < 2; ++m)
#pragma unroll
    for (int n = 0; n < 5; ++n) acc[m][n] = f32x4{0.f, 0.f, 0.f, 0.f};
  const int q4 = fr >> 2, p4 = fr & 3;
  if (wave < 8) {
    const int aoff0 = (wr * 32 + fr) * 128 + ((g ^ (fr & 7)) * 16);
    const int aoff1 = (wr * 32 + fr) * 128 + (((4 + g) ^ (fr & 7)) * 16);
    const int boff0 = K1_AB + (g * 10 + wc * 5) * 256 + ((q4 ^ (4 * (g & 1))) * 32) + 8 * p4;
    const int boff1 = K1_AB + (g * 10 + wc * 5) * 256 + (((4 + q4) ^ (4 * (g & 1))) * 32) + 8 * p4;
#pragma unroll 2
    for (int t = 0; t < K1_NSTEP; ++t) {
      __builtin_amdgcn_s_barrier();
      const char* buf = smem + (t & (K1_NBUF - 1)) * K1_ST;
#pragma unroll
      for (int kk = 0; kk < 2; ++kk) {
        bf16x8 a[2], b[5];
#pragma unroll
        for (int m = 0; m < 2; ++m) a[m] = *(const bf16x8*)(buf + (kk ? aoff1 : aoff0) + m * 2048);
#pragma unroll
        for (int n = 0; n < 5; ++n) {
          s16x4 lo = tr_read(buf + boff0 + kk * 40 * 256 + n * 256);
          s16x4 hi = tr_read(buf + boff1 + kk * 40 * 256 + n * 256);
          b[n] = __builtin_shufflevector(lo, hi, 0, 1, 2, 3, 4, 5, 6, 7);
        }
#pragma unroll
        for (int m = 0; m < 2; ++m)
#pragma unroll
          for (int n = 0; n < 5; ++n) acc[m][n] = MFMA16(b[n], a[m], acc[m][n]);
      }
    }
  }
  STAMPA(4);
  char* ep = smem + 2 * K1_ST;
  if (wave < 8)
#pragma unroll
  for (int m = 0; m < 2; ++m) {
    char* dst = ep + (wr * 32 + m * 16 + fr) * ERS + (wc * 80 + 4 * g) * 2;
#pragma unroll
    for (int n = 0; n < 5; ++n)
      *(u32x2*)(dst + 32 * n) = u32x2{pk2(acc[m][n][0], acc[m][n][1]), pk2(acc[m][n][2], acc[m][n][3])};
  }
  __syncthreads();
  STAMPA(1);
  if (isq) {
#pragma unroll 1
    for (int i = tid; i < 2560; i += 640) {
      const int row = i / 20, c = i - row * 20;
      u32x4 v = *(const u32x4*)(ep + row * ERS + c * 16);
      v[0] = phi_pk(v[0]); v[1] = phi_pk(v[1]); v[2] = phi_pk(v[2]); v[3] = phi_pk(v[3]);
      *(u32x4*)(pq + (size_t)(r0 + row) * D_ + 160 * cg + c * 8) = v;
    }
  } else {
#pragma unroll 1
    for (int i = tid; i < 1280; i += 640) {
      const int row = i / 10, c5 = i - row * 10;
      const int c = c5 < 5 ? c5 : c5 + 5;
      u32x4* p = (u32x4*)(ep + row * ERS + c * 16);
      u32x4 v = *p;
      v[0] = phi_pk(v[0]); v[1] = phi_pk(v[1]); v[2] = phi_pk(v[2]); v[3] = phi_pk(v[3]);
      *p = v;
    }
    __syncthreads();
    STAMPA(2);
    if (wave >= 8) return;
    const int hh = wave >> 1, dt = wave & 1;
    const int kcol = (hh >> 1) * 80 + (hh & 1) * 20;
    const char* abase = ep + (8 * g + q4) * ERS + (kcol + 16 * dt + 4 * p4) * 2;
    const char* bbase = ep + (8 * g + q4) * ERS + (kcol + 40 + 4 * p4) * 2;
    f32x4 c0 = f32x4{0.f, 0.f, 0.f, 0.f}, c1 = f32x4{0.f, 0.f, 0.f, 0.f};
    const bf16x8 ones = bf16x8{0x3f80, 0x3f80, 0x3f80, 0x3f80, 0x3f80, 0x3f80, 0x3f80, 0x3f80};
#pragma unroll 1
    for (int ks = 0; ks < 4; ++ks) {
      const int o = ks * 32 * ERS;
      s16x4 a0 = tr_read(abase + o), a1 = tr_read(abase + o + 4 * ERS);
      s16x4 b0 = tr_read(bbase + o), b1 = tr_read(bbase + o + 4 * ERS);
      s16x4 b2 = tr_read(bbase + o + 32), b3 = tr_read(bbase + o + 32 + 4 * ERS);
      bf16x8 a = __builtin_shufflevector(a0, a1, 0, 1, 2, 3, 4, 5, 6, 7);
      bf16x8 bA = __builtin_shufflevector(b0, b1, 0, 1, 2, 3, 4, 5, 6, 7);
      bf16x8 bB = __builtin_shufflevector(b2, b3, 0, 1, 2, 3, 4, 5, 6, 7);
      if (fr == 4) bB = ones;
      c0 = MFMA16(a, bA, c0);
      c1 = MFMA16(a, bB, c1);
    }
    float* pb = part + (size_t)(rt * NH + cg * 4 + hh) * 420;
#pragma unroll
    for (int r = 0; r < 4; ++r) {
      const int d = 16 * dt + 4 * g + r;
      if (d < 20) {
        pb[d * 20 + fr] = c0[r];
        if (fr < 4) pb[d * 20 + 16 + fr] = c1[r];
        if (fr == 4) pb[400 + d] = c1[r];
      }
    }
  }
}

typedef __attribute__((ext_vector_type(4), aligned(8))) unsigned u32x4a8;
__global__ __launch_bounds__(256) void k2_o(const u16* __restrict__ pq, const float* __restrict__ part,
                                            u16* __restrict__ o) {
  __shared__ __attribute__((aligned(16))) float red[448];
  const int tid = threadIdx.x, lane = tid & 63, wave = tid >> 6, fr = lane & 15, g = lane >> 4;
  const int h = blockIdx.x & 31, rc = blockIdx.x >> 5;
  u32x4 pb[4];
  const u16* src = pq + (size_t)(rc * 256 + wave * 64 + fr) * D_ + h * DH + 8 * (g < 3 ? g : 0);
#pragma unroll
  for (int u = 0; u < 4; ++u) pb[u] = *(const u32x4a8*)(src + u * 16 * D_);
  if (tid < 105) {
    const f32x4* pp = (const f32x4*)(part + (size_t)h * 420) + tid;
    f32x4 sum = f32x4{0.f, 0.f, 0.f, 0.f};
#pragma unroll
    for (int p = 0; p < NPART; ++p) sum += pp[(size_t)p * NH * 105];
    *(f32x4*)(red + tid * 4) = sum;
  }
  __syncthreads();
  unsigned t0[4], t1[4];
#pragma unroll
  for (int j2 = 0; j2 < 4; ++j2) {
    float a0[2], a1[2];
#pragma unroll
    for (int jj = 0; jj < 2; ++jj) {
      const int d = 8 * g + 2 * j2 + jj;
      const int dd = d < 20 ? d : 0;
      float v0 = red[dd * 20 + fr];
      float v1 = fr < 4 ? red[dd * 20 + 16 + fr] : red[400 + dd];
      a0[jj] = d < 20 ? v0 : 0.f;
      a1[jj] = (d < 20 && fr <= 4) ? v1 : 0.f;
    }
    t0[j2] = pk2(a0[0], a0[1]);
    t1[j2] = pk2(a1[0], a1[1]);
  }
  const bf16x8 T0 = __builtin_bit_cast(bf16x8, (u32x4{t0[0], t0[1], t0[2], t0[3]}));
  const bf16x8 T1 = __builtin_bit_cast(bf16x8, (u32x4{t1[0], t1[1], t1[2], t1[3]}));
#pragma unroll
  for (int u = 0; u < 4; ++u) {
    u32x4 b = pb[u];
    if (g == 2) { b[2] = 0u; b[3] = 0u; }
    if (g == 3) b = u32x4{0u, 0u, 0u, 0u};
    const bf16x8 bb = __builtin_bit_cast(bf16x8, b);
    f32x4 z4 = f32x4{0.f, 0.f, 0.f, 0.f};
    f32x4 n0 = MFMA16(T0, bb, z4);
    f32x4 n1 = MFMA16(T1, bb, z4);
    float den = __shfl(n1[0], 16 + fr);
    den = rbf(rbf(den) + 1e-6f);
    const float rd = __builtin_amdgcn_rcpf(den);
    const unsigned p01 = pk2(n0[0], n0[1]), p23 = pk2(n0[2], n0[3]);
    const unsigned q01 = pk2(n1[0], n1[1]), q23 = pk2(n1[2], n1[3]);
    u16* dst = o + (size_t)(rc * 256 + wave * 64 + u * 16 + fr) * D_ + h * DH;
    *(u32x2*)(dst + 4 * g) = u32x2{pk2(lo16(p01) * rd, hi16(p01) * rd), pk2(lo16(p23) * rd, hi16(p23) * rd)};
    if (g == 0) *(u32x2*)(dst + 16) = u32x2{pk2(lo16(q01) * rd, hi16(q01) * rd), pk2(lo16(q23) * rd, hi16(q23) * rd)};
  }
}

constexpr int K3_AB = 64 * 128 * 2;
constexpr int K3_BB = 128 * 80 * 2;
constexpr int K3_ST = K3_AB + K3_BB;
constexpr int K3_NSTEP = D_ / 128;
__global__ __launch_bounds__(640) void k3_out(const u16* __restrict__ o, const u16* __restrict__ wo,
                                              u16* __restrict__ out) {
  __shared__ __attribute__((aligned(16))) char smem[4 * K3_ST];
  const int tid = threadIdx.x, lane = tid & 63, fr = lane & 15, g = lane >> 4;
  const int wave = __builtin_amdgcn_readfirstlane(tid >> 6);
  const int bid = blockIdx.x, xcd = bid & 7, loc = bid >> 3;
  const int rt = xcd * 4 + (loc & 3), ct = loc >> 2;
  const int r0 = rt * 64, n0 = ct * 80;
  STAMPB(0);
  if (wave == 8) {
    const u16* gp[16];
#pragma unroll
    for (int it = 0; it < 16; ++it) {
      int i = it * 64 + lane;
      int row = i >> 4;
      int c = (i & 15) ^ (row & 15);
      gp[it] = o + (size_t)(r0 + row) * D_ + c * 8;
    }
    auto stage = [&](int t) {
      char* base = smem + (t & 3) * K3_ST;
#pragma unroll
      for (int it = 0; it < 16; ++it) glds16(gp[it] + t * 128, base + it * 1024);
    };
    stage(0); stage(1); stage(2);
    WAITV(32); __builtin_amdgcn_s_barrier(); stage(3);
    WAITV(32); __builtin_amdgcn_s_barrier(); stage(4);
    WAITV(32); __builtin_amdgcn_s_barrier();
    WAITV(16); __builtin_amdgcn_s_barrier();
    WAITV(0); __builtin_amdgcn_s_barrier();
    return;
  }
  if (wave == 9) {
    const u16* gp[20];
#pragma unroll
    for (int it = 0; it < 20; ++it) {
      int i = it * 64 + lane;
      int half = i & 1, rp = (i >> 1) & 7, tile = i >> 4;
      int kb = tile / 5, nt = tile - kb * 5;
      int krow = kb * 8 + (rp ^ (4 * (kb & 1)));
      gp[it] = wo + (size_t)krow * D_ + n0 + nt * 16 + half * 8;
    }
    auto stage = [&](int t) {
      char* base = smem + (t & 3) * K3_ST + K3_AB;
#pragma unroll
      for (int it = 0; it < 20; ++it) glds16(gp[it] + (size_t)t * 128 * D_, base + it * 1024);
    };
    stage(0); stage(1); stage(2);
    WAITV(40); __builtin_amdgcn_s_barrier(); stage(3);
    WAITV(40); __builtin_amdgcn_s_barrier(); stage(4);
    WAITV(40); __builtin_amdgcn_s_barrier();
    WAITV(20); __builtin_amdgcn_s_barrier();
    WAITV(0); __builtin_amdgcn_s_barrier();
    return;
  }
  const int rtile = wave & 3, cgp = wave >> 2;
  const int q4 = fr >> 2, p4 = fr & 3;
  const int arow = rtile * 16 + fr;
  const int boff0 = K3_AB + (g * 5 + cgp * 3) * 256 + ((q4 ^ (4 * (g & 1))) * 32) + 8 * p4;
  const int boff1 = K3_AB + (g * 5 + cgp * 3) * 256 + (((4 + q4) ^ (4 * (g & 1))) * 32) + 8 * p4;
  f32x4 acc[3];
#pragma unroll
  for (int n = 0; n < 3; ++n) acc[n] = f32x4{0.f, 0.f, 0.f, 0.f};
#pragma unroll
  for (int t = 0; t < K3_NSTEP; ++t) {
    __builtin_amdgcn_s_barrier();
    if (t == 0) STAMPB(1);
    const char* buf = smem + (t & 3) * K3_ST;
#pragma unroll
    for (int kk = 0; kk < 4; ++kk) {
      const bf16x8 a = *(const bf16x8*)(buf + arow * 256 + (((4 * kk + g) ^ fr) * 16));
#pragma unroll
      for (int n = 0; n < 3; ++n) {
        if (n < 2 || cgp == 0) {
          s16x4 lo = tr_read(buf + boff0 + kk * 20 * 256 + n * 256), hi = tr_read(buf + boff1 + kk * 20 * 256 + n * 256);
          bf16x8 b = __builtin_shufflevector(lo, hi, 0, 1, 2, 3, 4, 5, 6, 7);
          acc[n] = MFMA16(b, a, acc[n]);
        }
      }
    }
  }
  STAMPB(4);
  u16* dst = out + (size_t)(r0 + arow) * D_ + n0 + cgp * 48 + 4 * g;
#pragma unroll
  for (int n = 0; n < 3; ++n)
    if (n < 2 || cgp == 0) *(u32x2*)(dst + 16 * n) = u32x2{pk2(acc[n][0], acc[n][1]), pk2(acc[n][2], acc[n][3])};
}

extern "C" void kernel_launch(void* const* d_in, const int* in_sizes, int n_in,
                              void* d_out, int out_size, void* d_ws, size_t ws_size,
                              hipStream_t stream) {
  const u16* x = (const u16*)d_in[0];
  const u16* w = (const u16*)d_in[1];
  const u16* wo = (const u16*)d_in[2];
  char* ws = (char*)d_ws;
  u16* pq = (u16*)ws;
  float* part = (float*)(ws + 2621440);
  u16* o = (u16*)(ws + 2621440 + 860160);
  k1_qkv<<<192, 640, 0, stream>>>(x, w, pq, part);
  k2_o<<<256, 256, 0, stream>>>(pq, part, o);
  k3_out<<<256, 640, 0, stream>>>(o, wo, (u16*)d_out);
}
```

```cpp
#include <hip/hip_runtime.h>
#include <hip/hip_bf16.h>
#include <stdint.h>

typedef __attribute__((ext_vector_type(8))) short bf16x8;
typedef __attribute__((ext_vector_type(4))) short s16x4;
typedef __attribute__((ext_vector_type(4))) float f32x4;
typedef __attribute__((ext_vector_type(4))) unsigned u32x4;
typedef __attribute__((ext_vector_type(2))) unsigned u32x2;
typedef __attribute__((address_space(3))) s16x4 lds_s16x4;
typedef unsigned short u16;

#define DI __device__ __forceinline__
#define MFMA16(a, b, c) __builtin_amdgcn_mfma_f32_16x16x32_bf16((a), (b), (c), 0, 0, 0)
#define STAMPA(i)
#define STAMPB(i)

constexpr int S_ = 2048, D_ = 640, N3 = 1920, NH = 32, DH = 20;

DI u16 f2bf(float f) { __bf16 b = (__bf16)f; return __builtin_bit_cast(u16, b); }
DI float bf2f(u16 u) { return __uint_as_float(((unsigned)u) << 16); }
DI float rbf(float f) { return bf2f(f2bf(f)); }
DI unsigned pk2(float lo, float hi) { return (unsigned)f2bf(lo) | ((unsigned)f2bf(hi) << 16); }
DI float lo16(unsigned u) { return __uint_as_float(u << 16); }
DI float hi16(unsigned u) { return __uint_as_float(u & 0xffff0000u); }
DI float phi_t(float t) {
  float a = t > 0.f ? (t + 1.0f) : __expf(t);
  return rbf(a) + 1e-6f;
}
DI unsigned phi_pk(unsigned u) { return pk2(phi_t(lo16(u)), phi_t(hi16(u))); }
DI s16x4 tr_read(const char* p) {
  return __builtin_amdgcn_ds_read_tr16_b64_v4i16((lds_s16x4*)(p));
}
DI void glds16(const void* g, char* l) {
  __builtin_amdgcn_global_load_lds((const unsigned*)g, (unsigned*)l, 16, 0, 0);
}
#define WAITV(n) asm volatile("s_waitcnt vmcnt(" #n ")" ::: "memory")

constexpr int K1_AB = 128 * 64 * 2;
constexpr int K1_BB = 64 * 160 * 2;
constexpr int K1_ST = K1_AB + K1_BB;
constexpr int K1_NBUF = 4;
constexpr int K1_NSTEP = D_ / 64;
constexpr int NPART = 16;
constexpr int ERS = 352;

__global__ __launch_bounds__(640) void k1_qkv(const u16* __restrict__ x, const u16* __restrict__ w,
                                              u16* __restrict__ pq, float* __restrict__ part) {
  __shared__ __attribute__((aligned(16))) char smem[K1_NBUF * K1_ST];
  const int bid = blockIdx.x, xcd = bid & 7, loc = bid >> 3;
  const int rt = (xcd & 3) * 4 + (loc & 3);
  const int cgi = (xcd >> 2) * 6 + (loc >> 2);
  const bool isq = cgi >= 8;
  const int cg = isq ? cgi - 8 : cgi;

  const int tid = threadIdx.x, lane = tid & 63;
  const int wave = __builtin_amdgcn_readfirstlane(tid >> 6);
  const int wr = (wave >> 1) & 3, wc = wave & 1, fr = lane & 15, g = lane >> 4;
  const int r0 = rt * 128;

  STAMPA(0);
  if (wave == 8) {
    const u16* gp[16];
#pragma unroll
    for (int it = 0; it < 16; ++it) {
      int i = it * 64 + lane;
      int row = i >> 3;
      int c = (i & 7) ^ (row & 7);
      gp[it] = x + (size_t)(r0 + row) * D_ + c * 8;
    }
    auto stage = [&](int t) {
      char* base = smem + (t & (K1_NBUF - 1)) * K1_ST;
#pragma unroll
      for (int it = 0; it < 16; ++it) glds16(gp[it] + t * 64, base + it * 1024);
    };
    stage(0); stage(1); stage(2);
#pragma unroll 1
    for (int t = 0; t < K1_NSTEP - 2; ++t) {
      WAITV(32);
      __builtin_amdgcn_s_barrier();
      if (t + 3 < K1_NSTEP) stage(t + 3); else { WAITV(16); }
    }
    __builtin_amdgcn_s_barrier();
    WAITV(0);
    __builtin_amdgcn_s_barrier();
  } else if (wave == 9) {
    const u16* gp[20];
#pragma unroll
    for (int it = 0; it < 20; ++it) {
      int i = it * 64 + lane;
      int half = i & 1, rp = (i >> 1) & 7, tile = i >> 4;
      int kb = tile / 10, nt = tile - kb * 10;
      int krow = kb * 8 + (rp ^ (4 * (kb & 1)));
      int c = 2 * nt + half;
      int seg = c / 5, wi = c - seg * 5;
      int col = isq ? (160 * cg + 8 * c) : (((seg & 1) ? 1280 : 640) + 80 * cg + (seg >> 1) * 40 + 8 * wi);
      gp[it] = w + (size_t)krow * N3 + col;
    }
    auto stage = [&](int t) {
      char* base = smem + (t & (K1_NBUF - 1)) * K1_ST + K1_AB;
#pragma unroll
      for (int it = 0; it < 20; ++it) glds16(gp[it] + (size_t)t * 64 * N3, base + it * 1024);
    };
    stage(0); stage(1); stage(2);
#pragma unroll 1
    for (int t = 0; t < K1_NSTEP - 2; ++t) {
      WAITV(40);
      __builtin_amdgcn_s_barrier();
      if (t + 3 < K1_NSTEP) stage(t + 3); else { WAITV(20); }
    }
    __builtin_amdgcn_s_barrier();
    WAITV(0);
    __builtin_amdgcn_s_barrier();
  }
  f32x4 acc[2][5];
#pragma unroll
  for (int m = 0; m < 2; ++m)
#pragma unroll
    for (int n = 0; n < 5; ++n) acc[m][n] = f32x4{0.f, 0.f, 0.f, 0.f};
  const int q4 = fr >> 2, p4 = fr & 3;
  if (wave < 8) {
    const int aoff0 = (wr * 32 + fr) * 128 + ((g ^ (fr & 7)) * 16);
    const int aoff1 = (wr * 32 + fr) * 128 + (((4 + g) ^ (fr & 7)) * 16);
    const int boff0 = K1_AB + (g * 10 + wc * 5) * 256 + ((q4 ^ (4 * (g & 1))) * 32) + 8 * p4;
    const int boff1 = K1_AB + (g * 10 + wc * 5) * 256 + (((4 + q4) ^ (4 * (g & 1))) * 32) + 8 * p4;
#pragma unroll 2
    for (int t = 0; t < K1_NSTEP; ++t) {
      __builtin_amdgcn_s_barrier();
      const char* buf = smem + (t & (K1_NBUF - 1)) * K1_ST;
#pragma unroll
      for (int kk = 0; kk < 2; ++kk) {
        bf16x8 a[2], b[5];
#pragma unroll
        for (int m = 0; m < 2; ++m) a[m] = *(const bf16x8*)(buf + (kk ? aoff1 : aoff0) + m * 2048);
#pragma unroll
        for (int n = 0; n < 5; ++n) {
          s16x4 lo = tr_read(buf + boff0 + kk * 40 * 256 + n * 256);
          s16x4 hi = tr_read(buf + boff1 + kk * 40 * 256 + n * 256);
          b[n] = __builtin_shufflevector(lo, hi, 0, 1, 2, 3, 4, 5, 6, 7);
        }
#pragma unroll
        for (int m = 0; m < 2; ++m)
#pragma unroll
          for (int n = 0; n < 5; ++n) acc[m][n] = MFMA16(b[n], a[m], acc[m][n]);
      }
    }
  }
  STAMPA(4);
  char* ep = smem + 2 * K1_ST;
  if (wave < 8) {
#pragma unroll
    for (int m = 0; m < 2; ++m) {
      char* dst = ep + (wr * 32 + m * 16 + fr) * ERS + (wc * 80 + 4 * g) * 2;
#pragma unroll
      for (int n = 0; n < 5; ++n) {
        unsigned u0 = pk2(acc[m][n][0], acc[m][n][1]), u1 = pk2(acc[m][n][2], acc[m][n][3]);
        if (n < 3 || isq) {
          const unsigned f0 = phi_pk(u0), f1 = phi_pk(u1);
          const bool ap = isq || n < 2 || g < 2;
          u0 = ap ? f0 : u0; u1 = ap ? f1 : u1;
        }
        *(u32x2*)(dst + 32 * n) = u32x2{u0, u1};
      }
    }
  }
  __syncthreads();
  STAMPA(1);
  if (isq) {
#pragma unroll
    for (int it = 0; it < 4; ++it) {
      const int i = it * 640 + tid;
      const int row = i / 20, c = i - row * 20;
      *(u32x4*)(pq + (size_t)(r0 + row) * D_ + 160 * cg + c * 8) = *(const u32x4*)(ep + row * ERS + c * 16);
    }
  } else {
    if (wave >= 8) return;
    const int hh = wave >> 1, dt = wave & 1;
    const int kcol = (hh >> 1) * 80 + (hh & 1) * 20;
    const char* abase = ep + (8 * g + q4) * ERS + (kcol + 16 * dt + 4 * p4) * 2;
    const char* bbase = ep + (8 * g + q4) * ERS + (kcol + 40 + 4 * p4) * 2;
    f32x4 c0 = f32x4{0.f, 0.f, 0.f, 0.f}, c1 = f32x4{0.f, 0.f, 0.f, 0.f};
    const bf16x8 ones = bf16x8{0x3f80, 0x3f80, 0x3f80, 0x3f80, 0x3f80, 0x3f80, 0x3f80, 0x3f80};
#pragma unroll 1
    for (int ks = 0; ks < 4; ++ks) {
      const int o = ks * 32 * ERS;
      s16x4 a0 = tr_read(abase + o), a1 = tr_read(abase + o + 4 * ERS);
      s16x4 b0 = tr_read(bbase + o), b1 = tr_read(bbase + o + 4 * ERS);
      s16x4 b2 = tr_read(bbase + o + 32), b3 = tr_read(bbase + o + 32 + 4 * ERS);
      bf16x8 a = __builtin_shufflevector(a0, a1, 0, 1, 2, 3, 4, 5, 6, 7);
      bf16x8 bA = __builtin_shufflevector(b0, b1, 0, 1, 2, 3, 4, 5, 6, 7);
      bf16x8 bB = __builtin_shufflevector(b2, b3, 0, 1, 2, 3, 4, 5, 6, 7);
      if (fr == 4) bB = ones;
      c0 = MFMA16(a, bA, c0);
      c1 = MFMA16(a, bB, c1);
    }
    float* pb = part + (size_t)(rt * NH + cg * 4 + hh) * 420;
#pragma unroll
    for (int r = 0; r < 4; ++r) {
      const int d = 16 * dt + 4 * g + r;
      if (d < 20) {
        pb[d * 20 + fr] = c0[r];
        if (fr < 4) pb[d * 20 + 16 + fr] = c1[r];
        if (fr == 4) pb[400 + d] = c1[r];
      }
    }
  }
}

typedef __attribute__((ext_vector_type(4), aligned(8))) unsigned u32x4a8;
__global__ __launch_bounds__(256) void k2_o(const u16* __restrict__ pq, const float* __restrict__ part,
                                            u16* __restrict__ o) {
  __shared__ __attribute__((aligned(16))) float red[448];
  const int tid = threadIdx.x, lane = tid & 63, wave = tid >> 6, fr = lane & 15, g = lane >> 4;
  const int h = blockIdx.x & 31, rc = blockIdx.x >> 5;
  u32x4 pb[4];
  const u16* src = pq + (size_t)(rc * 256 + wave * 64 + fr) * D_ + h * DH + 8 * (g < 3 ? g : 0);
#pragma unroll
  for (int u = 0; u < 4; ++u) pb[u] = *(const u32x4a8*)(src + u * 16 * D_);
  if (tid < 105) {
    const f32x4* pp = (const f32x4*)(part + (size_t)h * 420) + tid;
    f32x4 sum = f32x4{0.f, 0.f, 0.f, 0.f};
#pragma unroll
    for (int p = 0; p < NPART; ++p) sum += pp[(size_t)p * NH * 105];
    *(f32x4*)(red + tid * 4) = sum;
  }
  __syncthreads();
  unsigned t0[4], t1[4];
#pragma unroll
  for (int j2 = 0; j2 < 4; ++j2) {
    float a0[2], a1[2];
#pragma unroll
    for (int jj = 0; jj < 2; ++jj) {
      const int d = 8 * g + 2 * j2 + jj;
      const int dd = d < 20 ? d : 0;
      float v0 = red[dd * 20 + fr];
      float v1 = fr < 4 ? red[dd * 20 + 16 + fr] : red[400 + dd];
      a0[jj] = d < 20 ? v0 : 0.f;
      a1[jj] = (d < 20 && fr <= 4) ? v1 : 0.f;
    }
    t0[j2] = pk2(a0[0], a0[1]);
    t1[j2] = pk2(a1[0], a1[1]);
  }
  const bf16x8 T0 = __builtin_bit_cast(bf16x8, (u32x4{t0[0], t0[1], t0[2], t0[3]}));
  const bf16x8 T1 = __builtin_bit_cast(bf16x8, (u32x4{t1[0], t1[1], t1[2], t1[3]}));
#pragma unroll
  for (int u = 0; u < 4; ++u) {
    u32x4 b = pb[u];
    if (g == 2) { b[2] = 0u; b[3] = 0u; }
    if (g == 3) b = u32x4{0u, 0u, 0u, 0u};
    const bf16x8 bb = __builtin_bit_cast(bf16x8, b);
    f32x4 z4 = f32x4{0.f, 0.f, 0.f, 0.f};
    f32x4 n0 = MFMA16(T0, bb, z4);
    f32x4 n1 = MFMA16(T1, bb, z4);
    float den = __shfl(n1[0], 16 + fr);
    den = rbf(rbf(den) + 1e-6f);
    const float rd = __builtin_amdgcn_rcpf(den);
    const unsigned p01 = pk2(n0[0], n0[1]), p23 = pk2(n0[2], n0[3]);
    const unsigned q01 = pk2(n1[0], n1[1]), q23 = pk2(n1[2], n1[3]);
    u16* dst = o + (size_t)(rc * 256 + wave * 64 + u * 16 + fr) * D_ + h * DH;
    *(u32x2*)(dst + 4 * g) = u32x2{pk2(lo16(p01) * rd, hi16(p01) * rd), pk2(lo16(p23) * rd, hi16(p23) * rd)};
    if (g == 0) *(u32x2*)(dst + 16) = u32x2{pk2(lo16(q01) * rd, hi16(q01) * rd), pk2(lo16(q23) * rd, hi16(q23) * rd)};
  }
}

constexpr int K3_AB = 64 * 128 * 2;
constexpr int K3_BB = 128 * 80 * 2;
constexpr int K3_ST = K3_AB + K3_BB;
constexpr int K3_NSTEP = D_ / 128;
__global__ __launch_bounds__(640) void k3_out(const u16* __restrict__ o, const u16* __restrict__ wo,
                                              u16* __restrict__ out) {
  __shared__ __attribute__((aligned(16))) char smem[4 * K3_ST];
  const int tid = threadIdx.x, lane = tid & 63, fr = lane & 15, g = lane >> 4;
  const int wave = __builtin_amdgcn_readfirstlane(tid >> 6);
  const int bid = blockIdx.x, xcd = bid & 7, loc = bid >> 3;
  const int rt = xcd * 4 + (loc & 3), ct = loc >> 2;
  const int r0 = rt * 64, n0 = ct * 80;
  STAMPB(0);
  if (wave == 8) {
    const u16* gp[16];
#pragma unroll
    for (int it = 0; it < 16; ++it) {
      int i = it * 64 + lane;
      int row = i >> 4;
      int c = (i & 15) ^ (row & 15);
      gp[it] = o + (size_t)(r0 + row) * D_ + c * 8;
    }
    auto stage = [&](int t) {
      char* base = smem + (t & 3) * K3_ST;
#pragma unroll
      for (int it = 0; it < 16; ++it) glds16(gp[it] + t * 128, base + it * 1024);
    };
    stage(0); stage(1); stage(2);
    WAITV(32); __builtin_amdgcn_s_barrier(); stage(3);
    WAITV(32); __builtin_amdgcn_s_barrier(); stage(4);
    WAITV(32); __builtin_amdgcn_s_barrier();
    WAITV(16); __builtin_amdgcn_s_barrier();
    WAITV(0); __builtin_amdgcn_s_barrier();
    return;
  }
  if (wave == 9) {
    const u16* gp[20];
#pragma unroll
    for (int it = 0; it < 20; ++it) {
      int i = it * 64 + lane;
      int half = i & 1, rp = (i >> 1) & 7, tile = i >> 4;
      int kb = tile / 5, nt = tile - kb * 5;
      int krow = kb * 8 + (rp ^ (4 * (kb & 1)));
      gp[it] = wo + (size_t)krow * D_ + n0 + nt * 16 + half * 8;
    }
    auto stage = [&](int t) {
      char* base = smem + (t & 3) * K3_ST + K3_AB;
#pragma unroll
      for (int it = 0; it < 20; ++it) glds16(gp[it] + (size_t)t * 128 * D_, base + it * 1024);
    };
    stage(0); stage(1); stage(2);
    WAITV(40); __builtin_amdgcn_s_barrier(); stage(3);
    WAITV(40); __builtin_amdgcn_s_barrier(); stage(4);
    WAITV(40); __builtin_amdgcn_s_barrier();
    WAITV(20); __builtin_amdgcn_s_barrier();
    WAITV(0); __builtin_amdgcn_s_barrier();
    return;
  }
  const int rtile = wave & 3, cgp = wave >> 2;
  const int q4 = fr >> 2, p4 = fr & 3;
  const int arow = rtile * 16 + fr;
  const int boff0 = K3_AB + (g * 5 + cgp * 3) * 256 + ((q4 ^ (4 * (g & 1))) * 32) + 8 * p4;
  const int boff1 = K3_AB + (g * 5 + cgp * 3) * 256 + (((4 + q4) ^ (4 * (g & 1))) * 32) + 8 * p4;
  f32x4 acc[3];
#pragma unroll
  for (int n = 0; n < 3; ++n) acc[n] = f32x4{0.f, 0.f, 0.f, 0.f};
#pragma unroll
  for (int t = 0; t < K3_NSTEP; ++t) {
    __builtin_amdgcn_s_barrier();
    if (t == 0) STAMPB(1);
    const char* buf = smem + (t & 3) * K3_ST;
#pragma unroll
    for (int kk = 0; kk < 4; ++kk) {
      const bf16x8 a = *(const bf16x8*)(buf + arow * 256 + (((4 * kk + g) ^ fr) * 16));
#pragma unroll
      for (int n = 0; n < 3; ++n) {
        if (n < 2 || cgp == 0) {
          s16x4 lo = tr_read(buf + boff0 + kk * 20 * 256 + n * 256), hi = tr_read(buf + boff1 + kk * 20 * 256 + n * 256);
          bf16x8 b = __builtin_shufflevector(lo, hi, 0, 1, 2, 3, 4, 5, 6, 7);
          acc[n] = MFMA16(b, a, acc[n]);
        }
      }
    }
  }
  STAMPB(4);
  u16* dst = out + (size_t)(r0 + arow) * D_ + n0 + cgp * 48 + 4 * g;
#pragma unroll
  for (int n = 0; n < 3; ++n)
    if (n < 2 || cgp == 0) *(u32x2*)(dst + 16 * n) = u32x2{pk2(acc[n][0], acc[n][1]), pk2(acc[n][2], acc[n][3])};
}

extern "C" void kernel_launch(void* const* d_in, const int* in_sizes, int n_in,
                              void* d_out, int out_size, void* d_ws, size_t ws_size,
                              hipStream_t stream) {
  const u16* x = (const u16*)d_in[0];
  const u16* w = (const u16*)d_in[1];
  const u16* wo = (const u16*)d_in[2];
  char* ws = (char*)d_ws;
  u16* pq = (u16*)ws;
  float* part = (float*)(ws + 2621440);
  u16* o = (u16*)(ws + 2621440 + 860160);
  k1_qkv<<<192, 640, 0, stream>>>(x, w, pq, part);
  k2_o<<<256, 256, 0, stream>>>(pq, part, o);
  k3_out<<<256, 640, 0, stream>>>(o, wo, (u16*)d_out);
}
```

```cpp
#include <hip/hip_runtime.h>
#include <hip/hip_bf16.h>
#include <stdint.h>

typedef __attribute__((ext_vector_type(8))) short bf16x8;
typedef __attribute__((ext_vector_type(4))) short s16x4;
typedef __attribute__((ext_vector_type(4))) float f32x4;
typedef __attribute__((ext_vector_type(4))) unsigned u32x4;
typedef __attribute__((ext_vector_type(2))) unsigned u32x2;
typedef __attribute__((address_space(3))) s16x4 lds_s16x4;
typedef unsigned short u16;

#define DI __device__ __forceinline__
#define MFMA16(a, b, c) __builtin_amdgcn_mfma_f32_16x16x32_bf16((a), (b), (c), 0, 0, 0)
#define STAMPA(i)
#define STAMPB(i)

constexpr int S_ = 2048, D_ = 640, N3 = 1920, NH = 32, DH = 20;

DI u16 f2bf(float f) { __bf16 b = (__bf16)f; return __builtin_bit_cast(u16, b); }
DI float bf2f(u16 u) { return __uint_as_float(((unsigned)u) << 16); }
DI float rbf(float f) { return bf2f(f2bf(f)); }
DI unsigned pk2(float lo, float hi) { return (unsigned)f2bf(lo) | ((unsigned)f2bf(hi) << 16); }
DI float lo16(unsigned u) { return __uint_as_float(u << 16); }
DI float hi16(unsigned u) { return __uint_as_float(u & 0xffff0000u); }
DI float phi_t(float t) {
  float a = t > 0.f ? (t + 1.0f) : __expf(t);
  return rbf(a) + 1e-6f;
}
DI unsigned phi_pk(unsigned u) { return pk2(phi_t(lo16(u)), phi_t(hi16(u))); }
DI s16x4 tr_read(const char* p) {
  return __builtin_amdgcn_ds_read_tr16_b64_v4i16((lds_s16x4*)(p));
}
DI void glds16(const void* g, char* l) {
  __builtin_amdgcn_global_load_lds((const unsigned*)g, (unsigned*)l, 16, 0, 0);
}
#define WAITV(n) asm volatile("s_waitcnt vmcnt(" #n ")" ::: "memory")

constexpr int K1_AB = 128 * 64 * 2;
constexpr int K1_BB = 64 * 160 * 2;
constexpr int K1_ST = K1_AB + K1_BB;
constexpr int K1_NBUF = 4;
constexpr int K1_NSTEP = D_ / 64;
constexpr int NPART = 16;
constexpr int ERS = 352;

__global__ __launch_bounds__(640) void k1_qkv(const u16* __restrict__ x, const u16* __restrict__ w,
                                              u16* __restrict__ pq, float* __restrict__ part) {
  __shared__ __attribute__((aligned(16))) char smem[K1_NBUF * K1_ST];
  const int bid = blockIdx.x, xcd = bid & 7, loc = bid >> 3;
  const int rt = (xcd & 3) * 4 + (loc & 3);
  const int cgi = (xcd >> 2) * 6 + (loc >> 2);
  const bool isq = cgi >= 8;
  const int cg = isq ? cgi - 8 : cgi;

  const int tid = threadIdx.x, lane = tid & 63;
  const int wave = __builtin_amdgcn_readfirstlane(tid >> 6);
  const int wr = (wave >> 1) & 3, wc = wave & 1, fr = lane & 15, g = lane >> 4;
  const int r0 = rt * 128;

  STAMPA(0);
  if (wave == 8) {
    const u16* gp[16];
#pragma unroll
    for (int it = 0; it < 16; ++it) {
      int i = it * 64 + lane;
      int row = i >> 3;
      int c = (i & 7) ^ (row & 7);
      gp[it] = x + (size_t)(r0 + row) * D_ + c * 8;
    }
    auto stage = [&](int t) {
      char* base = smem + (t & (K1_NBUF - 1)) * K1_ST;
#pragma unroll
      for (int it = 0; it < 16; ++it) glds16(gp[it] + t * 64, base + it * 1024);
    };
    stage(0); stage(1); stage(2);
#pragma unroll 1
    for (int t = 0; t < K1_NSTEP - 2; ++t) {
      WAITV(32);
      __builtin_amdgcn_s_barrier();
      if (t + 3 < K1_NSTEP) stage(t + 3); else { WAITV(16); }
    }
    __builtin_amdgcn_s_barrier();
    WAITV(0);
    __builtin_amdgcn_s_barrier();
  } else if (wave == 9) {
    const u16* gp[20];
#pragma unroll
    for (int it = 0; it < 20; ++it) {
      int i = it * 64 + lane;
      int half = i & 1, rp = (i >> 1) & 7, tile = i >> 4;
      int kb = tile / 10, nt = tile - kb * 10;
      int krow = kb * 8 + (rp ^ (4 * (kb & 1)));
      int c = 2 * nt + half;
      int seg = c / 5, wi = c - seg * 5;
      int col = isq ? (160 * cg + 8 * c) : (((seg & 1) ? 1280 : 640) + 80 * cg + (seg >> 1) * 40 + 8 * wi);
      gp[it] = w + (size_t)krow * N3 + col;
    }
    auto stage = [&](int t) {
      char* base = smem + (t & (K1_NBUF - 1)) * K1_ST + K1_AB;
#pragma unroll
      for (int it = 0; it < 20; ++it) glds16(gp[it] + (size_t)t * 64 * N3, base + it * 1024);
    };
    stage(0); stage(1); stage(2);
#pragma unroll 1
    for (int t = 0; t < K1_NSTEP - 2; ++t) {
      WAITV(40);
      __builtin_amdgcn_s_barrier();
      if (t + 3 < K1_NSTEP) stage(t + 3); else { WAITV(20); }
    }
    __builtin_amdgcn_s_barrier();
    WAITV(0);
    __builtin_amdgcn_s_barrier();
  }
  f32x4 acc[2][5];
#pragma unroll
  for (int m = 0; m < 2; ++m)
#pragma unroll
    for (int n = 0; n < 5; ++n) acc[m][n] = f32x4{0.f, 0.f, 0.f, 0.f};
  const int q4 = fr >> 2, p4 = fr & 3;
  unsigned pfd[2] = {0u, 0u};
  if (wave < 8) {
#pragma unroll
    for (int j = 0; j < 2; ++j) {
      const int id = (wave * 2 + j) * 64 + lane;
      const char* pa;
      if (id < 640) {
        const int row = (loc & 3) * 160 + (id >> 2), wh = id & 3;
        int cb;
        if (isq) cb = (160 * cg) * 2 + (wh == 3 ? 318 : wh * 128);
        else cb = (((wh & 2) ? 1280 : 640) + 80 * cg) * 2 + ((wh & 1) ? 158 : 0);
        pa = (const char*)w + (size_t)row * (N3 * 2) + cb;
      } else {
        int l = (loc >> 2) + 6 * (id - 640);
        l = l < 1280 ? l : 1279;
        pa = (const char*)x + (size_t)r0 * (D_ * 2) + (size_t)l * 128;
      }
      pa = (const char*)((size_t)pa & ~(size_t)3);
      asm volatile("global_load_dword %0, %1, off" : "=v"(pfd[j]) : "v"(pa) : "memory");
    }
  }
  if (wave < 8) {
    const int aoff0 = (wr * 32 + fr) * 128 + ((g ^ (fr & 7)) * 16);
    const int aoff1 = (wr * 32 + fr) * 128 + (((4 + g) ^ (fr & 7)) * 16);
    const int boff0 = K1_AB + (g * 10 + wc * 5) * 256 + ((q4 ^ (4 * (g & 1))) * 32) + 8 * p4;
    const int boff1 = K1_AB + (g * 10 + wc * 5) * 256 + (((4 + q4) ^ (4 * (g & 1))) * 32) + 8 * p4;
#pragma unroll 2
    for (int t = 0; t < K1_NSTEP; ++t) {
      __builtin_amdgcn_s_barrier();
      const char* buf = smem + (t & (K1_NBUF - 1)) * K1_ST;
#pragma unroll
      for (int kk = 0; kk < 2; ++kk) {
        bf16x8 a[2], b[5];
#pragma unroll
        for (int m = 0; m < 2; ++m) a[m] = *(const bf16x8*)(buf + (kk ? aoff1 : aoff0) + m * 2048);
#pragma unroll
        for (int n = 0; n < 5; ++n) {
          s16x4 lo = tr_read(buf + boff0 + kk * 40 * 256 + n * 256);
          s16x4 hi = tr_read(buf + boff1 + kk * 40 * 256 + n * 256);
          b[n] = __builtin_shufflevector(lo, hi, 0, 1, 2, 3, 4, 5, 6, 7);
        }
#pragma unroll
        for (int m = 0; m < 2; ++m)
#pragma unroll
          for (int n = 0; n < 5; ++n) acc[m][n] = MFMA16(b[n], a[m], acc[m][n]);
      }
    }
  }
  STAMPA(4);
  asm volatile("s_waitcnt vmcnt(0)" :: "v"(pfd[0]), "v"(pfd[1]) : "memory");
  char* ep = smem + 2 * K1_ST;
  if (wave < 8) {
#pragma unroll
    for (int m = 0; m < 2; ++m) {
      char* dst = ep + (wr * 32 + m * 16 + fr) * ERS + (wc * 80 + 4 * g) * 2;
#pragma unroll
      for (int n = 0; n < 5; ++n) {
        unsigned u0 = pk2(acc[m][n][0], acc[m][n][1]), u1 = pk2(acc[m][n][2], acc[m][n][3]);
        if (n < 3 || isq) {
          const unsigned f0 = phi_pk(u0), f1 = phi_pk(u1);
          const bool ap = isq || n < 2 || g < 2;
          u0 = ap ? f0 : u0; u1 = ap ? f1 : u1;
        }
        *(u32x2*)(dst + 32 * n) = u32x2{u0, u1};
      }
    }
  }
  __syncthreads();
  STAMPA(1);
  if (isq) {
#pragma unroll
    for (int it = 0; it < 4; ++it) {
      const int i = it * 640 + tid;
      const int row = i / 20, c = i - row * 20;
      *(u32x4*)(pq + (size_t)(r0 + row) * D_ + 160 * cg + c * 8) = *(const u32x4*)(ep + row * ERS + c * 16);
    }
  } else {
    if (wave >= 8) return;
    const int hh = wave >> 1, dt = wave & 1;
    const int kcol = (hh >> 1) * 80 + (hh & 1) * 20;
    const char* abase = ep + (8 * g + q4) * ERS + (kcol + 16 * dt + 4 * p4) * 2;
    const char* bbase = ep + (8 * g + q4) * ERS + (kcol + 40 + 4 * p4) * 2;
    f32x4 c0 = f32x4{0.f, 0.f, 0.f, 0.f}, c1 = f32x4{0.f, 0.f, 0.f, 0.f};
    const bf16x8 ones = bf16x8{0x3f80, 0x3f80, 0x3f80, 0x3f80, 0x3f80, 0x3f80, 0x3f80, 0x3f80};
#pragma unroll 1
    for (int ks = 0; ks < 4; ++ks) {
      const int o = ks * 32 * ERS;
      s16x4 a0 = tr_read(abase + o), a1 = tr_read(abase + o + 4 * ERS);
      s16x4 b0 = tr_read(bbase + o), b1 = tr_read(bbase + o + 4 * ERS);
      s16x4 b2 = tr_read(bbase + o + 32), b3 = tr_read(bbase + o + 32 + 4 * ERS);
      bf16x8 a = __builtin_shufflevector(a0, a1, 0, 1, 2, 3, 4, 5, 6, 7);
      bf16x8 bA = __builtin_shufflevector(b0, b1, 0, 1, 2, 3, 4, 5, 6, 7);
      bf16x8 bB = __builtin_shufflevector(b2, b3, 0, 1, 2, 3, 4, 5, 6, 7);
      if (fr == 4) bB = ones;
      c0 = MFMA16(a, bA, c0);
      c1 = MFMA16(a, bB, c1);
    }
    float* pb = part + (size_t)(rt * NH + cg * 4 + hh) * 420;
#pragma unroll
    for (int r = 0; r < 4; ++r) {
      const int d = 16 * dt + 4 * g + r;
      if (d < 20) {
        pb[d * 20 + fr] = c0[r];
        if (fr < 4) pb[d * 20 + 16 + fr] = c1[r];
        if (fr == 4) pb[400 + d] = c1[r];
      }
    }
  }
}

typedef __attribute__((ext_vector_type(4), aligned(8))) unsigned u32x4a8;
__global__ __launch_bounds__(256) void k2_o(const u16* __restrict__ pq, const float* __restrict__ part,
                                            u16* __restrict__ o) {
  __shared__ __attribute__((aligned(16))) float red[448];
  const int tid = threadIdx.x, lane = tid & 63, wave = tid >> 6, fr = lane & 15, g = lane >> 4;
  const int h = blockIdx.x & 31, rc = blockIdx.x >> 5;
  u32x4 pb[4];
  const u16* src = pq + (size_t)(rc * 256 + wave * 64 + fr) * D_ + h * DH + 8 * (g < 3 ? g : 0);
#pragma unroll
  for (int u = 0; u < 4; ++u) pb[u] = *(const u32x4a8*)(src + u * 16 * D_);
  if (tid < 105) {
    const f32x4* pp = (const f32x4*)(part + (size_t)h * 420) + tid;
    f32x4 sum = f32x4{0.f, 0.f, 0.f, 0.f};
#pragma unroll
    for (int p = 0; p < NPART; ++p) sum += pp[(size_t)p * NH * 105];
    *(f32x4*)(red + tid * 4) = sum;
  }
  __syncthreads();
  unsigned t0[4], t1[4];
#pragma unroll
  for (int j2 = 0; j2 < 4; ++j2) {
    float a0[2], a1[2];
#pragma unroll
    for (int jj = 0; jj < 2; ++jj) {
      const int d = 8 * g + 2 * j2 + jj;
      const int dd = d < 20 ? d : 0;
      float v0 = red[dd * 20 + fr];
      float v1 = fr < 4 ? red[dd * 20 + 16 + fr] : red[400 + dd];
      a0[jj] = d < 20 ? v0 : 0.f;
      a1[jj] = (d < 20 && fr <= 4) ? v1 : 0.f;
    }
    t0[j2] = pk2(a0[0], a0[1]);
    t1[j2] = pk2(a1[0], a1[1]);
  }
  const bf16x8 T0 = __builtin_bit_cast(bf16x8, (u32x4{t0[0], t0[1], t0[2], t0[3]}));
  const bf16x8 T1 = __builtin_bit_cast(bf16x8, (u32x4{t1[0], t1[1], t1[2], t1[3]}));
#pragma unroll
  for (int u = 0; u < 4; ++u) {
    u32x4 b = pb[u];
    if (g == 2) { b[2] = 0u; b[3] = 0u; }
    if (g == 3) b = u32x4{0u, 0u, 0u, 0u};
    const bf16x8 bb = __builtin_bit_cast(bf16x8, b);
    f32x4 z4 = f32x4{0.f, 0.f, 0.f, 0.f};
    f32x4 n0 = MFMA16(T0, bb, z4);
    f32x4 n1 = MFMA16(T1, bb, z4);
    float den = __shfl(n1[0], 16 + fr);
    den = rbf(rbf(den) + 1e-6f);
    const float rd = __builtin_amdgcn_rcpf(den);
    const unsigned p01 = pk2(n0[0], n0[1]), p23 = pk2(n0[2], n0[3]);
    const unsigned q01 = pk2(n1[0], n1[1]), q23 = pk2(n1[2], n1[3]);
    u16* dst = o + (size_t)(rc * 256 + wave * 64 + u * 16 + fr) * D_ + h * DH;
    *(u32x2*)(dst + 4 * g) = u32x2{pk2(lo16(p01) * rd, hi16(p01) * rd), pk2(lo16(p23) * rd, hi16(p23) * rd)};
    if (g == 0) *(u32x2*)(dst + 16) = u32x2{pk2(lo16(q01) * rd, hi16(q01) * rd), pk2(lo16(q23) * rd, hi16(q23) * rd)};
  }
}

constexpr int K3_AB = 64 * 128 * 2;
constexpr int K3_BB = 128 * 80 * 2;
constexpr int K3_ST = K3_AB + K3_BB;
constexpr int K3_NSTEP = D_ / 128;
__global__ __launch_bounds__(640) void k3_out(const u16* __restrict__ o, const u16* __restrict__ wo,
                                              u16* __restrict__ out) {
  __shared__ __attribute__((aligned(16))) char smem[4 * K3_ST];
  const int tid = threadIdx.x, lane = tid & 63, fr = lane & 15, g = lane >> 4;
  const int wave = __builtin_amdgcn_readfirstlane(tid >> 6);
  const int bid = blockIdx.x, xcd = bid & 7, loc = bid >> 3;
  const int rt = xcd * 4 + (loc & 3), ct = loc >> 2;
  const int r0 = rt * 64, n0 = ct * 80;
  STAMPB(0);
  if (wave == 8) {
    const u16* gp[16];
#pragma unroll
    for (int it = 0; it < 16; ++it) {
      int i = it * 64 + lane;
      int row = i >> 4;
      int c = (i & 15) ^ (row & 15);
      gp[it] = o + (size_t)(r0 + row) * D_ + c * 8;
    }
    auto stage = [&](int t) {
      char* base = smem + (t & 3) * K3_ST;
#pragma unroll
      for (int it = 0; it < 16; ++it) glds16(gp[it] + t * 128, base + it * 1024);
    };
    stage(0); stage(1); stage(2);
    WAITV(32); __builtin_amdgcn_s_barrier(); stage(3);
    WAITV(32); __builtin_amdgcn_s_barrier(); stage(4);
    WAITV(32); __builtin_amdgcn_s_barrier();
    WAITV(16); __builtin_amdgcn_s_barrier();
    WAITV(0); __builtin_amdgcn_s_barrier();
    return;
  }
  if (wave == 9) {
    const u16* gp[20];
#pragma unroll
    for (int it = 0; it < 20; ++it) {
      int i = it * 64 + lane;
      int half = i & 1, rp = (i >> 1) & 7, tile = i >> 4;
      int kb = tile / 5, nt = tile - kb * 5;
      int krow = kb * 8 + (rp ^ (4 * (kb & 1)));
      gp[it] = wo + (size_t)krow * D_ + n0 + nt * 16 + half * 8;
    }
    auto stage = [&](int t) {
      char* base = smem + (t & 3) * K3_ST + K3_AB;
#pragma unroll
      for (int it = 0; it < 20; ++it) glds16(gp[it] + (size_t)t * 128 * D_, base + it * 1024);
    };
    stage(0); stage(1); stage(2);
    WAITV(40); __builtin_amdgcn_s_barrier(); stage(3);
    WAITV(40); __builtin_amdgcn_s_barrier(); stage(4);
    WAITV(40); __builtin_amdgcn_s_barrier();
    WAITV(20); __builtin_amdgcn_s_barrier();
    WAITV(0); __builtin_amdgcn_s_barrier();
    return;
  }
  unsigned pfd;
  {
    const int id = wave * 64 + lane;
    const char* pa;
    if (id < 200) pa = (const char*)wo + (size_t)(loc * 200 + id) * 128;
    else { const int l = id - 200 < 80 ? id - 200 : 79; pa = (const char*)o + (size_t)r0 * (D_ * 2) + (size_t)(ct * 80 + l) * 128; }
    asm volatile("global_load_dword %0, %1, off" : "=v"(pfd) : "v"(pa) : "memory");
  }
  const int rtile = wave & 3, cgp = wave >> 2;
  const int q4 = fr >> 2, p4 = fr & 3;
  const int arow = rtile * 16 + fr;
  const int boff0 = K3_AB + (g * 5 + cgp * 3) * 256 + ((q4 ^ (4 * (g & 1))) * 32) + 8 * p4;
  const int boff1 = K3_AB + (g * 5 + cgp * 3) * 256 + (((4 + q4) ^ (4 * (g & 1))) * 32) + 8 * p4;
  f32x4 acc[3];
#pragma unroll
  for (int n = 0; n < 3; ++n) acc[n] = f32x4{0.f, 0.f, 0.f, 0.f};
#pragma unroll
  for (int t = 0; t < K3_NSTEP; ++t) {
    __builtin_amdgcn_s_barrier();
    if (t == 0) STAMPB(1);
    const char* buf = smem + (t & 3) * K3_ST;
#pragma unroll
    for (int kk = 0; kk < 4; ++kk) {
      const bf16x8 a = *(const bf16x8*)(buf + arow * 256 + (((4 * kk + g) ^ fr) * 16));
#pragma unroll
      for (int n = 0; n < 3; ++n) {
        if (n < 2 || cgp == 0) {
          s16x4 lo = tr_read(buf + boff0 + kk * 20 * 256 + n * 256), hi = tr_read(buf + boff1 + kk * 20 * 256 + n * 256);
          bf16x8 b = __builtin_shufflevector(lo, hi, 0, 1, 2, 3, 4, 5, 6, 7);
          acc[n] = MFMA16(b, a, acc[n]);
        }
      }
    }
  }
  STAMPB(4);
  asm volatile("s_waitcnt vmcnt(0)" :: "v"(pfd) : "memory");
  u16* dst = out + (size_t)(r0 + arow) * D_ + n0 + cgp * 48 + 4 * g;
#pragma unroll
  for (int n = 0; n < 3; ++n)
    if (n < 2 || cgp == 0) *(u32x2*)(dst + 16 * n) = u32x2{pk2(acc[n][0], acc[n][1]), pk2(acc[n][2], acc[n][3])};
}

extern "C" void kernel_launch(void* const* d_in, const int* in_sizes, int n_in,
                              void* d_out, int out_size, void* d_ws, size_t ws_size,
                              hipStream_t stream) {
  const u16* x = (const u16*)d_in[0];
  const u16* w = (const u16*)d_in[1];
  const u16* wo = (const u16*)d_in[2];
  char* ws = (char*)d_ws;
  u16* pq = (u16*)ws;
  float* part = (float*)(ws + 2621440);
  u16* o = (u16*)(ws + 2621440 + 860160);
  k1_qkv<<<192, 640, 0, stream>>>(x, w, pq, part);
  k2_o<<<256, 256, 0, stream>>>(pq, part, o);
  k3_out<<<256, 640, 0, stream>>>(o, wo, (u16*)d_out);
}
```

```cpp
#include <hip/hip_runtime.h>
#include <hip/hip_bf16.h>
#include <stdint.h>

typedef __attribute__((ext_vector_type(8))) short bf16x8;
typedef __attribute__((ext_vector_type(4))) short s16x4;
typedef __attribute__((ext_vector_type(4))) float f32x4;
typedef __attribute__((ext_vector_type(4))) unsigned u32x4;
typedef __attribute__((ext_vector_type(2))) unsigned u32x2;
typedef __attribute__((address_space(3))) s16x4 lds_s16x4;
typedef unsigned short u16;

#define DI __device__ __forceinline__
#define MFMA16(a, b, c) __builtin_amdgcn_mfma_f32_16x16x32_bf16((a), (b), (c), 0, 0, 0)
#define STAMPA(i)
#define STAMPB(i)

constexpr int S_ = 2048, D_ = 640, N3 = 1920, NH = 32, DH = 20;

DI u16 f2bf(float f) { __bf16 b = (__bf16)f; return __builtin_bit_cast(u16, b); }
DI float bf2f(u16 u) { return __uint_as_float(((unsigned)u) << 16); }
DI float rbf(float f) { return bf2f(f2bf(f)); }
DI unsigned pk2(float lo, float hi) { return (unsigned)f2bf(lo) | ((unsigned)f2bf(hi) << 16); }
DI float lo16(unsigned u) { return __uint_as_float(u << 16); }
DI float hi16(unsigned u) { return __uint_as_float(u & 0xffff0000u); }
DI float phi_t(float t) {
  float a = t > 0.f ? (t + 1.0f) : __expf(t);
  return rbf(a) + 1e-6f;
}
DI unsigned phi_pk(unsigned u) { return pk2(phi_t(lo16(u)), phi_t(hi16(u))); }
DI s16x4 tr_read(const char* p) {
  return __builtin_amdgcn_ds_read_tr16_b64_v4i16((lds_s16x4*)(p));
}
DI void glds16(const void* g, char* l) {
  __builtin_amdgcn_global_load_lds((const unsigned*)g, (unsigned*)l, 16, 0, 0);
}
#define WAITV(n) asm volatile("s_waitcnt vmcnt(" #n ")" ::: "memory")

constexpr int K1_AB = 128 * 64 * 2;
constexpr int K1_BB = 64 * 160 * 2;
constexpr int K1_ST = K1_AB + K1_BB;
constexpr int K1_NBUF = 4;
constexpr int K1_NSTEP = D_ / 64;
constexpr int NPART = 16;
constexpr int ERS = 352;

__global__ __launch_bounds__(640) void k1_qkv(const u16* __restrict__ x, const u16* __restrict__ w,
                                              u16* __restrict__ pq, float* __restrict__ part) {
  __shared__ __attribute__((aligned(16))) char smem[K1_NBUF * K1_ST];
  const int bid = blockIdx.x, xcd = bid & 7, loc = bid >> 3;
  const int rt = (xcd & 3) * 4 + (loc & 3);
  const int cgi = (xcd >> 2) * 6 + (loc >> 2);
  const bool isq = cgi >= 8;
  const int cg = isq ? cgi - 8 : cgi;

  const int tid = threadIdx.x, lane = tid & 63;
  const int wave = __builtin_amdgcn_readfirstlane(tid >> 6);
  const int wr = (wave >> 1) & 3, wc = wave & 1, fr = lane & 15, g = lane >> 4;
  const int r0 = rt * 128;

  STAMPA(0);
  if (wave == 8) {
    const u16* gp[16];
#pragma unroll
    for (int it = 0; it < 16; ++it) {
      int i = it * 64 + lane;
      int row = i >> 3;
      int c = (i & 7) ^ (row & 7);
      gp[it] = x + (size_t)(r0 + row) * D_ + c * 8;
    }
    auto stage = [&](int t) {
      char* base = smem + (t & (K1_NBUF - 1)) * K1_ST;
#pragma unroll
      for (int it = 0; it < 16; ++it) glds16(gp[it] + t * 64, base + it * 1024);
    };
    stage(0); stage(1); stage(2);
#pragma unroll 1
    for (int t = 0; t < K1_NSTEP - 2; ++t) {
      WAITV(32);
      __builtin_amdgcn_s_barrier();
      if (t + 3 < K1_NSTEP) stage(t + 3); else { WAITV(16); }
    }
    __builtin_amdgcn_s_barrier();
    WAITV(0);
    __builtin_amdgcn_s_barrier();
  } else if (wave == 9) {
    const u16* gp[20];
#pragma unroll
    for (int it = 0; it < 20; ++it) {
      int i = it * 64 + lane;
      int half = i & 1, rp = (i >> 1) & 7, tile = i >> 4;
      int kb = tile / 10, nt = tile - kb * 10;
      int krow = kb * 8 + (rp ^ (4 * (kb & 1)));
      int c = 2 * nt + half;
      int seg = c / 5, wi = c - seg * 5;
      int col = isq ? (160 * cg + 8 * c) : (((seg & 1) ? 1280 : 640) + 80 * cg + (seg >> 1) * 40 + 8 * wi);
      gp[it] = w + (size_t)krow * N3 + col;
    }
    auto stage = [&](int t) {
      char* base = smem + (t & (K1_NBUF - 1)) * K1_ST + K1_AB;
#pragma unroll
      for (int it = 0; it < 20; ++it) glds16(gp[it] + (size_t)t * 64 * N3, base + it * 1024);
    };
    stage(0); stage(1); stage(2);
#pragma unroll 1
    for (int t = 0; t < K1_NSTEP - 2; ++t) {
      WAITV(40);
      __builtin_amdgcn_s_barrier();
      if (t + 3 < K1_NSTEP) stage(t + 3); else { WAITV(20); }
    }
    __builtin_amdgcn_s_barrier();
    WAITV(0);
    __builtin_amdgcn_s_barrier();
  }
  f32x4 acc[2][5];
#pragma unroll
  for (int m = 0; m < 2; ++m)
#pragma unroll
    for (int n = 0; n < 5; ++n) acc[m][n] = f32x4{0.f, 0.f, 0.f, 0.f};
  const int q4 = fr >> 2, p4 = fr & 3;
  unsigned pfd[2] = {0u, 0u};
  if (wave < 8) {
#pragma unroll
    for (int j = 0; j < 2; ++j) {
      const int id = (wave * 2 + j) * 64 + lane;
      const char* pa;
      if (id < 640) {
        const int row = (loc & 3) * 160 + (id >> 2), wh = id & 3;
        int cb;
        if (isq) cb = (160 * cg) * 2 + (wh == 3 ? 318 : wh * 128);
        else cb = (((wh & 2) ? 1280 : 640) + 80 * cg) * 2 + ((wh & 1) ? 158 : 0);
        pa = (const char*)w + (size_t)row * (N3 * 2) + cb;
      } else {
        int l = (loc >> 2) + 6 * (id - 640);
        l = l < 1280 ? l : 1279;
        pa = (const char*)x + (size_t)r0 * (D_ * 2) + (size_t)l * 128;
      }
      pa = (const char*)((size_t)pa & ~(size_t)3);
      asm volatile("global_load_dword %0, %1, off" : "=v"(pfd[j]) : "v"(pa) : "memory");
    }
  }
  if (wave < 8) {
    const int aoff0 = (wr * 32 + fr) * 128 + ((g ^ (fr & 7)) * 16);
    const int aoff1 = (wr * 32 + fr) * 128 + (((4 + g) ^ (fr & 7)) * 16);
    const int boff0 = K1_AB + (g * 10 + wc * 5) * 256 + ((q4 ^ (4 * (g & 1))) * 32) + 8 * p4;
    const int boff1 = K1_AB + (g * 10 + wc * 5) * 256 + (((4 + q4) ^ (4 * (g & 1))) * 32) + 8 * p4;
#pragma unroll 2
    for (int t = 0; t < K1_NSTEP; ++t) {
      __builtin_amdgcn_s_barrier();
      const char* buf = smem + (t & (K1_NBUF - 1)) * K1_ST;
#pragma unroll
      for (int kk = 0; kk < 2; ++kk) {
        bf16x8 a[2], b[5];
#pragma unroll
        for (int m = 0; m < 2; ++m) a[m] = *(const bf16x8*)(buf + (kk ? aoff1 : aoff0) + m * 2048);
#pragma unroll
        for (int n = 0; n < 5; ++n) {
          s16x4 lo = tr_read(buf + boff0 + kk * 40 * 256 + n * 256);
          s16x4 hi = tr_read(buf + boff1 + kk * 40 * 256 + n * 256);
          b[n] = __builtin_shufflevector(lo, hi, 0, 1, 2, 3, 4, 5, 6, 7);
        }
#pragma unroll
        for (int m = 0; m < 2; ++m)
#pragma unroll
          for (int n = 0; n < 5; ++n) acc[m][n] = MFMA16(b[n], a[m], acc[m][n]);
      }
    }
  }
  STAMPA(4);
  asm volatile("s_waitcnt vmcnt(0)" :: "v"(pfd[0]), "v"(pfd[1]) : "memory");
  char* ep = smem + 2 * K1_ST;
  if (wave < 8) {
#pragma unroll
    for (int m = 0; m < 2; ++m) {
      char* dst = ep + (wr * 32 + m * 16 + fr) * ERS + (wc * 80 + 4 * g) * 2;
#pragma unroll
      for (int n = 0; n < 5; ++n) {
        unsigned u0 = pk2(acc[m][n][0], acc[m][n][1]), u1 = pk2(acc[m][n][2], acc[m][n][3]);
        if (n < 3 || isq) {
          const unsigned f0 = phi_pk(u0), f1 = phi_pk(u1);
          const bool ap = isq || n < 2 || g < 2;
          u0 = ap ? f0 : u0; u1 = ap ? f1 : u1;
        }
        *(u32x2*)(dst + 32 * n) = u32x2{u0, u1};
      }
    }
  }
  __syncthreads();
  STAMPA(1);
  if (isq) {
#pragma unroll
    for (int it = 0; it < 4; ++it) {
      const int i = it * 640 + tid;
      const int row = i / 20, c = i - row * 20;
      *(u32x4*)(pq + (size_t)(r0 + row) * D_ + 160 * cg + c * 8) = *(const u32x4*)(ep + row * ERS + c * 16);
    }
  } else {
    if (wave >= 8) return;
    const int hh = wave >> 1, dt = wave & 1;
    const int kcol = (hh >> 1) * 80 + (hh & 1) * 20;
    const char* abase = ep + (8 * g + q4) * ERS + (kcol + 16 * dt + 4 * p4) * 2;
    const char* bbase = ep + (8 * g + q4) * ERS + (kcol + 40 + 4 * p4) * 2;
    f32x4 c0 = f32x4{0.f, 0.f, 0.f, 0.f}, c1 = f32x4{0.f, 0.f, 0.f, 0.f};
    const bf16x8 ones = bf16x8{0x3f80, 0x3f80, 0x3f80, 0x3f80, 0x3f80, 0x3f80, 0x3f80, 0x3f80};
#pragma unroll 1
    for (int ks = 0; ks < 4; ++ks) {
      const int o = ks * 32 * ERS;
      s16x4 a0 = tr_read(abase + o), a1 = tr_read(abase + o + 4 * ERS);
      s16x4 b0 = tr_read(bbase + o), b1 = tr_read(bbase + o + 4 * ERS);
      s16x4 b2 = tr_read(bbase + o + 32), b3 = tr_read(bbase + o + 32 + 4 * ERS);
      bf16x8 a = __builtin_shufflevector(a0, a1, 0, 1, 2, 3, 4, 5, 6, 7);
      bf16x8 bA = __builtin_shufflevector(b0, b1, 0, 1, 2, 3, 4, 5, 6, 7);
      bf16x8 bB = __builtin_shufflevector(b2, b3, 0, 1, 2, 3, 4, 5, 6, 7);
      if (fr == 4) bB = ones;
      c0 = MFMA16(a, bA, c0);
      c1 = MFMA16(a, bB, c1);
    }
    float* pb = part + (size_t)(rt * NH + cg * 4 + hh) * 420;
#pragma unroll
    for (int r = 0; r < 4; ++r) {
      const int d = 16 * dt + 4 * g + r;
      if (d < 20) {
        pb[d * 20 + fr] = c0[r];
        if (fr < 4) pb[d * 20 + 16 + fr] = c1[r];
        if (fr == 4) pb[400 + d] = c1[r];
      }
    }
  }
}

typedef __attribute__((ext_vector_type(4), aligned(8))) unsigned u32x4a8;
__global__ __launch_bounds__(256) void k2_o(const u16* __restrict__ pq, const float* __restrict__ part,
                                            u16* __restrict__ o, const u16* __restrict__ wo) {
  __shared__ __attribute__((aligned(16))) float red[448];
  const int tid = threadIdx.x, lane = tid & 63, wave = tid >> 6, fr = lane & 15, g = lane >> 4;
  const int h = blockIdx.x & 31, rc = blockIdx.x >> 5;
  u32x4 pb[4];
  const u16* src = pq + (size_t)(rc * 256 + wave * 64 + fr) * D_ + h * DH + 8 * (g < 3 ? g : 0);
#pragma unroll
  for (int u = 0; u < 4; ++u) pb[u] = *(const u32x4a8*)(src + u * 16 * D_);
  unsigned pfd = 0u;
  if (tid >= 56) {
    const char* pa = (const char*)wo + (size_t)((blockIdx.x >> 3) * 200 + tid - 56) * 128;
    asm volatile("global_load_dword %0, %1, off" : "=v"(pfd) : "v"(pa) : "memory");
  }
  if (tid < 105) {
    const f32x4* pp = (const f32x4*)(part + (size_t)h * 420) + tid;
    f32x4 sum = f32x4{0.f, 0.f, 0.f, 0.f};
#pragma unroll
    for (int p = 0; p < NPART; ++p) sum += pp[(size_t)p * NH * 105];
    *(f32x4*)(red + tid * 4) = sum;
  }
  __syncthreads();
  unsigned t0[4], t1[4];
#pragma unroll
  for (int j2 = 0; j2 < 4; ++j2) {
    float a0[2], a1[2];
#pragma unroll
    for (int jj = 0; jj < 2; ++jj) {
      const int d = 8 * g + 2 * j2 + jj;
      const int dd = d < 20 ? d : 0;
      float v0 = red[dd * 20 + fr];
      float v1 = fr < 4 ? red[dd * 20 + 16 + fr] : red[400 + dd];
      a0[jj] = d < 20 ? v0 : 0.f;
      a1[jj] = (d < 20 && fr <= 4) ? v1 : 0.f;
    }
    t0[j2] = pk2(a0[0], a0[1]);
    t1[j2] = pk2(a1[0], a1[1]);
  }
  const bf16x8 T0 = __builtin_bit_cast(bf16x8, (u32x4{t0[0], t0[1], t0[2], t0[3]}));
  const bf16x8 T1 = __builtin_bit_cast(bf16x8, (u32x4{t1[0], t1[1], t1[2], t1[3]}));
#pragma unroll
  for (int u = 0; u < 4; ++u) {
    u32x4 b = pb[u];
    if (g == 2) { b[2] = 0u; b[3] = 0u; }
    if (g == 3) b = u32x4{0u, 0u, 0u, 0u};
    const bf16x8 bb = __builtin_bit_cast(bf16x8, b);
    f32x4 z4 = f32x4{0.f, 0.f, 0.f, 0.f};
    f32x4 n0 = MFMA16(T0, bb, z4);
    f32x4 n1 = MFMA16(T1, bb, z4);
    float den = __shfl(n1[0], 16 + fr);
    den = rbf(rbf(den) + 1e-6f);
    const float rd = __builtin_amdgcn_rcpf(den);
    const unsigned p01 = pk2(n0[0], n0[1]), p23 = pk2(n0[2], n0[3]);
    const unsigned q01 = pk2(n1[0], n1[1]), q23 = pk2(n1[2], n1[3]);
    u16* dst = o + (size_t)(rc * 256 + wave * 64 + u * 16 + fr) * D_ + h * DH;
    *(u32x2*)(dst + 4 * g) = u32x2{pk2(lo16(p01) * rd, hi16(p01) * rd), pk2(lo16(p23) * rd, hi16(p23) * rd)};
    if (g == 0) *(u32x2*)(dst + 16) = u32x2{pk2(lo16(q01) * rd, hi16(q01) * rd), pk2(lo16(q23) * rd, hi16(q23) * rd)};
  }
  asm volatile("s_waitcnt vmcnt(0)" :: "v"(pfd) : "memory");
}

constexpr int K3_AB = 64 * 128 * 2;
constexpr int K3_BB = 128 * 80 * 2;
constexpr int K3_ST = K3_AB + K3_BB;
constexpr int K3_NSTEP = D_ / 128;
__global__ __launch_bounds__(640) void k3_out(const u16* __restrict__ o, const u16* __restrict__ wo,
                                              u16* __restrict__ out) {
  __shared__ __attribute__((aligned(16))) char smem[4 * K3_ST];
  const int tid = threadIdx.x, lane = tid & 63, fr = lane & 15, g = lane >> 4;
  const int wave = __builtin_amdgcn_readfirstlane(tid >> 6);
  const int bid = blockIdx.x, xcd = bid & 7, loc = bid >> 3;
  const int rt = xcd * 4 + (loc & 3), ct = loc >> 2;
  const int r0 = rt * 64, n0 = ct * 80;
  STAMPB(0);
  if (wave == 8) {
    const u16* gp[16];
#pragma unroll
    for (int it = 0; it < 16; ++it) {
      int i = it * 64 + lane;
      int row = i >> 4;
      int c = (i & 15) ^ (row & 15);
      gp[it] = o + (size_t)(r0 + row) * D_ + c * 8;
    }
    auto stage = [&](int t) {
      char* base = smem + (t & 3) * K3_ST;
#pragma unroll
      for (int it = 0; it < 16; ++it) glds16(gp[it] + t * 128, base + it * 1024);
    };
    stage(0); stage(1); stage(2);
    WAITV(32); __builtin_amdgcn_s_barrier(); stage(3);
    WAITV(32); __builtin_amdgcn_s_barrier(); stage(4);
    WAITV(32); __builtin_amdgcn_s_barrier();
    WAITV(16); __builtin_amdgcn_s_barrier();
    WAITV(0); __builtin_amdgcn_s_barrier();
    return;
  }
  if (wave == 9) {
    const u16* gp[20];
#pragma unroll
    for (int it = 0; it < 20; ++it) {
      int i = it * 64 + lane;
      int half = i & 1, rp = (i >> 1) & 7, tile = i >> 4;
      int kb = tile / 5, nt = tile - kb * 5;
      int krow = kb * 8 + (rp ^ (4 * (kb & 1)));
      gp[it] = wo + (size_t)krow * D_ + n0 + nt * 16 + half * 8;
    }
    auto stage = [&](int t) {
      char* base = smem + (t & 3) * K3_ST + K3_AB;
#pragma unroll
      for (int it = 0; it < 20; ++it) glds16(gp[it] + (size_t)t * 128 * D_, base + it * 1024);
    };
    stage(0); stage(1); stage(2);
    WAITV(40); __builtin_amdgcn_s_barrier(); stage(3);
    WAITV(40); __builtin_amdgcn_s_barrier(); stage(4);
    WAITV(40); __builtin_amdgcn_s_barrier();
    WAITV(20); __builtin_amdgcn_s_barrier();
    WAITV(0); __builtin_amdgcn_s_barrier();
    return;
  }
  unsigned pfd;
  {
    const int id = wave * 64 + lane;
    const char* pa;
    if (id < 200) pa = (const char*)wo + (size_t)(loc * 200 + id) * 128;
    else { const int l = id - 200 < 80 ? id - 200 : 79; pa = (const char*)o + (size_t)r0 * (D_ * 2) + (size_t)(ct * 80 + l) * 128; }
    asm volatile("global_load_dword %0, %1, off" : "=v"(pfd) : "v"(pa) : "memory");
  }
  const int rtile = wave & 3, cgp = wave >> 2;
  const int q4 = fr >> 2, p4 = fr & 3;
  const int arow = rtile * 16 + fr;
  const int boff0 = K3_AB + (g * 5 + cgp * 3) * 256 + ((q4 ^ (4 * (g & 1))) * 32) + 8 * p4;
  const int boff1 = K3_AB + (g * 5 + cgp * 3) * 256 + (((4 + q4) ^ (4 * (g & 1))) * 32) + 8 * p4;
  f32x4 acc[3];
#pragma unroll
  for (int n = 0; n < 3; ++n) acc[n] = f32x4{0.f, 0.f, 0.f, 0.f};
#pragma unroll
  for (int t = 0; t < K3_NSTEP; ++t) {
    __builtin_amdgcn_s_barrier();
    if (t == 0) STAMPB(1);
    const char* buf = smem + (t & 3) * K3_ST;
#pragma unroll
    for (int kk = 0; kk < 4; ++kk) {
      const bf16x8 a = *(const bf16x8*)(buf + arow * 256 + (((4 * kk + g) ^ fr) * 16));
#pragma unroll
      for (int n = 0; n < 3; ++n) {
        if (n < 2 || cgp == 0) {
          s16x4 lo = tr_read(buf + boff0 + kk * 20 * 256 + n * 256), hi = tr_read(buf + boff1 + kk * 20 * 256 + n * 256);
          bf16x8 b = __builtin_shufflevector(lo, hi, 0, 1, 2, 3, 4, 5, 6, 7);
          acc[n] = MFMA16(b, a, acc[n]);
        }
      }
    }
  }
  STAMPB(4);
  asm volatile("s_waitcnt vmcnt(0)" :: "v"(pfd) : "memory");
  u16* dst = out + (size_t)(r0 + arow) * D_ + n0 + cgp * 48 + 4 * g;
#pragma unroll
  for (int n = 0; n < 3; ++n)
    if (n < 2 || cgp == 0) *(u32x2*)(dst + 16 * n) = u32x2{pk2(acc[n][0], acc[n][1]), pk2(acc[n][2], acc[n][3])};
}

extern "C" void kernel_launch(void* const* d_in, const int* in_sizes, int n_in,
                              void* d_out, int out_size, void* d_ws, size_t ws_size,
                              hipStream_t stream) {
  const u16* x = (const u16*)d_in[0];
  const u16* w = (const u16*)d_in[1];
  const u16* wo = (const u16*)d_in[2];
  char* ws = (char*)d_ws;
  u16* pq = (u16*)ws;
  float* part = (float*)(ws + 2621440);
  u16* o = (u16*)(ws + 2621440 + 860160);
  k1_qkv<<<192, 640, 0, stream>>>(x, w, pq, part);
  k2_o<<<256, 256, 0, stream>>>(pq, part, o, wo);
  k3_out<<<256, 640, 0, stream>>>(o, wo, (u16*)d_out);
}
```

```cpp
#include <hip/hip_runtime.h>
#include <hip/hip_bf16.h>
#include <stdint.h>

typedef __attribute__((ext_vector_type(8))) short bf16x8;
typedef __attribute__((ext_vector_type(4))) short s16x4;
typedef __attribute__((ext_vector_type(4))) float f32x4;
typedef __attribute__((ext_vector_type(4))) unsigned u32x4;
typedef __attribute__((ext_vector_type(2))) unsigned u32x2;
typedef __attribute__((address_space(3))) s16x4 lds_s16x4;
typedef unsigned short u16;

#define DI __device__ __forceinline__
#define MFMA16(a, b, c) __builtin_amdgcn_mfma_f32_16x16x32_bf16((a), (b), (c), 0, 0, 0)

constexpr int S_ = 2048, D_ = 640, N3 = 1920, NH = 32, DH = 20;

DI u16 f2bf(float f) { __bf16 b = (__bf16)f; return __builtin_bit_cast(u16, b); }
DI float bf2f(u16 u) { return __uint_as_float(((unsigned)u) << 16); }
DI float rbf(float f) { return bf2f(f2bf(f)); }
DI unsigned pk2(float lo, float hi) { return (unsigned)f2bf(lo) | ((unsigned)f2bf(hi) << 16); }
DI float lo16(unsigned u) { return __uint_as_float(u << 16); }
DI float hi16(unsigned u) { return __uint_as_float(u & 0xffff0000u); }
DI float phi_t(float t) {
  float a = t > 0.f ? (t + 1.0f) : __expf(t);
  return rbf(a) + 1e-6f;
}
DI unsigned phi_pk(unsigned u) { return pk2(phi_t(lo16(u)), phi_t(hi16(u))); }
DI s16x4 tr_read(const char* p) {
  return __builtin_amdgcn_ds_read_tr16_b64_v4i16((lds_s16x4*)(p));
}
DI void glds16(const void* g, char* l) {
  __builtin_amdgcn_global_load_lds((const unsigned*)g, (unsigned*)l, 16, 0, 0);
}
#define WAITV(n) asm volatile("s_waitcnt vmcnt(" #n ")" ::: "memory")

constexpr int K1_AB = 128 * 64 * 2;
constexpr int K1_BB = 64 * 160 * 2;
constexpr int K1_ST = K1_AB + K1_BB;
constexpr int K1_NBUF = 4;
constexpr int K1_NSTEP = D_ / 64;
constexpr int NPART = 16;
constexpr int ERS = 352;

__global__ __launch_bounds__(640) void k1_qkv(const u16* __restrict__ x, const u16* __restrict__ w,
                                              u16* __restrict__ pq, float* __restrict__ part) {
  __shared__ __attribute__((aligned(16))) char smem[K1_NBUF * K1_ST];
  const int bid = blockIdx.x, xcd = bid & 7, loc = bid >> 3;
  const int rt = (xcd & 3) * 4 + (loc & 3);
  const int cgi = (xcd >> 2) * 6 + (loc >> 2);
  const bool isq = cgi >= 8;
  const int cg = isq ? cgi - 8 : cgi;

  const int tid = threadIdx.x, lane = tid & 63;
  const int wave = __builtin_amdgcn_readfirstlane(tid >> 6);
  const int wr = (wave >> 1) & 3, wc = wave & 1, fr = lane & 15, g = lane >> 4;
  const int r0 = rt * 128;

  if (wave == 8) {
    const u16* gp[16];
#pragma unroll
    for (int it = 0; it < 16; ++it) {
      int i = it * 64 + lane;
      int row = i >> 3;
      int c = (i & 7) ^ (row & 7);
      gp[it] = x + (size_t)(r0 + row) * D_ + c * 8;
    }
    auto stage = [&](int t) {
      char* base = smem + (t & (K1_NBUF - 1)) * K1_ST;
#pragma unroll
      for (int it = 0; it < 16; ++it) glds16(gp[it] + t * 64, base + it * 1024);
    };
    stage(0); stage(1); stage(2);
#pragma unroll 1
    for (int t = 0; t < K1_NSTEP - 2; ++t) {
      WAITV(32);
      __builtin_amdgcn_s_barrier();
      if (t + 3 < K1_NSTEP) stage(t + 3); else { WAITV(16); }
    }
    __builtin_amdgcn_s_barrier();
    WAITV(0);
    __builtin_amdgcn_s_barrier();
  } else if (wave == 9) {
    const u16* gp[20];
#pragma unroll
    for (int it = 0; it < 20; ++it) {
      int i = it * 64 + lane;
      int half = i & 1, rp = (i >> 1) & 7, tile = i >> 4;
      int kb = tile / 10, nt = tile - kb * 10;
      int krow = kb * 8 + (rp ^ (4 * (kb & 1)));
      int c = 2 * nt + half;
      int seg = c / 5, wi = c - seg * 5;
      int col = isq ? (160 * cg + 8 * c) : (((seg & 1) ? 1280 : 640) + 80 * cg + (seg >> 1) * 40 + 8 * wi);
      gp[it] = w + (size_t)krow * N3 + col;
    }
    auto stage = [&](int t) {
      char* base = smem + (t & (K1_NBUF - 1)) * K1_ST + K1_AB;
#pragma unroll
      for (int it = 0; it < 20; ++it) glds16(gp[it] + (size_t)t * 64 * N3, base + it * 1024);
    };
    stage(0); stage(1); stage(2);
#pragma unroll 1
    for (int t = 0; t < K1_NSTEP - 2; ++t) {
      WAITV(40);
      __builtin_amdgcn_s_barrier();
      if (t + 3 < K1_NSTEP) stage(t + 3); else { WAITV(20); }
    }
    __builtin_amdgcn_s_barrier();
    WAITV(0);
    __builtin_amdgcn_s_barrier();
  }
  f32x4 acc[2][5];
#pragma unroll
  for (int m = 0; m < 2; ++m)
#pragma unroll
    for (int n = 0; n < 5; ++n) acc[m][n] = f32x4{0.f, 0.f, 0.f, 0.f};
  const int q4 = fr >> 2, p4 = fr & 3;
  unsigned pfd[2] = {0u, 0u};
  if (wave < 8) {
#pragma unroll
    for (int j = 0; j < 2; ++j) {
      const int id = (wave * 2 + j) * 64 + lane;
      const char* pa;
      if (id < 640) {
        const int row = (loc & 3) * 160 + (id >> 2), wh = id & 3;
        int cb;
        if (isq) cb = (160 * cg) * 2 + (wh == 3 ? 318 : wh * 128);
        else cb = (((wh & 2) ? 1280 : 640) + 80 * cg) * 2 + ((wh & 1) ? 158 : 0);
        pa = (const char*)w + (size_t)row * (N3 * 2) + cb;
      } else {
        int l = (loc >> 2) + 6 * (id - 640);
        l = l < 1280 ? l : 1279;
        pa = (const char*)x + (size_t)r0 * (D_ * 2) + (size_t)l * 128;
      }
      pa = (const char*)((size_t)pa & ~(size_t)3);
      asm volatile("global_load_dword %0, %1, off" : "=v"(pfd[j]) : "v"(pa) : "memory");
    }
  }
  if (wave < 8) {
    const int aoff0 = (wr * 32 + fr) * 128 + ((g ^ (fr & 7)) * 16);
    const int aoff1 = (wr * 32 + fr) * 128 + (((4 + g) ^ (fr & 7)) * 16);
    const int boff0 = K1_AB + (g * 10 + wc * 5) * 256 + ((q4 ^ (4 * (g & 1))) * 32) + 8 * p4;
    const int boff1 = K1_AB + (g * 10 + wc * 5) * 256 + (((4 + q4) ^ (4 * (g & 1))) * 32) + 8 * p4;
#pragma unroll 2
    for (int t = 0; t < K1_NSTEP; ++t) {
      __builtin_amdgcn_s_barrier();
      const char* buf = smem + (t & (K1_NBUF - 1)) * K1_ST;
#pragma unroll
      for (int kk = 0; kk < 2; ++kk) {
        bf16x8 a[2], b[5];
#pragma unroll
        for (int m = 0; m < 2; ++m) a[m] = *(const bf16x8*)(buf + (kk ? aoff1 : aoff0) + m * 2048);
#pragma unroll
        for (int n = 0; n < 5; ++n) {
          s16x4 lo = tr_read(buf + boff0 + kk * 40 * 256 + n * 256);
          s16x4 hi = tr_read(buf + boff1 + kk * 40 * 256 + n * 256);
          b[n] = __builtin_shufflevector(lo, hi, 0, 1, 2, 3, 4, 5, 6, 7);
        }
#pragma unroll
        for (int m = 0; m < 2; ++m)
#pragma unroll
          for (int n = 0; n < 5; ++n) acc[m][n] = MFMA16(b[n], a[m], acc[m][n]);
      }
    }
  }
  asm volatile("s_waitcnt vmcnt(0)" :: "v"(pfd[0]), "v"(pfd[1]) : "memory");
  char* ep = smem + 2 * K1_ST;
  if (wave < 8) {
#pragma unroll
    for (int m = 0; m < 2; ++m) {
      char* dst = ep + (wr * 32 + m * 16 + fr) * ERS + (wc * 80 + 4 * g) * 2;
#pragma unroll
      for (int n = 0; n < 5; ++n) {
        unsigned u0 = pk2(acc[m][n][0], acc[m][n][1]), u1 = pk2(acc[m][n][2], acc[m][n][3]);
        if (n < 3 || isq) {
          const unsigned f0 = phi_pk(u0), f1 = phi_pk(u1);
          const bool ap = isq || n < 2 || g < 2;
          u0 = ap ? f0 : u0; u1 = ap ? f1 : u1;
        }
        *(u32x2*)(dst + 32 * n) = u32x2{u0, u1};
      }
    }
  }
  __syncthreads();
  if (isq) {
#pragma unroll
    for (int it = 0; it < 4; ++it) {
      const int i = it * 640 + tid;
      const int row = i / 20, c = i - row * 20;
      *(u32x4*)(pq + (size_t)(r0 + row) * D_ + 160 * cg + c * 8) = *(const u32x4*)(ep + row * ERS + c * 16);
    }
  } else {
    if (wave >= 8) return;
    const int hh = wave >> 1, dt = wave & 1;
    const int kcol = (hh >> 1) * 80 + (hh & 1) * 20;
    const char* abase = ep + (8 * g + q4) * ERS + (kcol + 16 * dt + 4 * p4) * 2;
    const char* bbase = ep + (8 * g + q4) * ERS + (kcol + 40 + 4 * p4) * 2;
    f32x4 c0 = f32x4{0.f, 0.f, 0.f, 0.f}, c1 = f32x4{0.f, 0.f, 0.f, 0.f};
    const bf16x8 ones = bf16x8{0x3f80, 0x3f80, 0x3f80, 0x3f80, 0x3f80, 0x3f80, 0x3f80, 0x3f80};
#pragma unroll 1
    for (int ks = 0; ks < 4; ++ks) {
      const int o = ks * 32 * ERS;
      s16x4 a0 = tr_read(abase + o), a1 = tr_read(abase + o + 4 * ERS);
      s16x4 b0 = tr_read(bbase + o), b1 = tr_read(bbase + o + 4 * ERS);
      s16x4 b2 = tr_read(bbase + o + 32), b3 = tr_read(bbase + o + 32 + 4 * ERS);
      bf16x8 a = __builtin_shufflevector(a0, a1, 0, 1, 2, 3, 4, 5, 6, 7);
      bf16x8 bA = __builtin_shufflevector(b0, b1, 0, 1, 2, 3, 4, 5, 6, 7);
      bf16x8 bB = __builtin_shufflevector(b2, b3, 0, 1, 2, 3, 4, 5, 6, 7);
      if (fr == 4) bB = ones;
      c0 = MFMA16(a, bA, c0);
      c1 = MFMA16(a, bB, c1);
    }
    float* pb = part + (size_t)(rt * NH + cg * 4 + hh) * 420;
#pragma unroll
    for (int r = 0; r < 4; ++r) {
      const int d = 16 * dt + 4 * g + r;
      if (d < 20) {
        pb[d * 20 + fr] = c0[r];
        if (fr < 4) pb[d * 20 + 16 + fr] = c1[r];
        if (fr == 4) pb[400 + d] = c1[r];
      }
    }
  }
}

typedef __attribute__((ext_vector_type(4), aligned(8))) unsigned u32x4a8;
__global__ __launch_bounds__(256) void k2_o(const u16* __restrict__ pq, const float* __restrict__ part,
                                            u16* __restrict__ o) {
  __shared__ __attribute__((aligned(16))) float red[448];
  const int tid = threadIdx.x, lane = tid & 63, wave = tid >> 6, fr = lane & 15, g = lane >> 4;
  const int h = blockIdx.x & 31, rc = blockIdx.x >> 5;
  u32x4 pb[4];
  const u16* src = pq + (size_t)(rc * 256 + wave * 64 + fr) * D_ + h * DH + 8 * (g < 3 ? g : 0);
#pragma unroll
  for (int u = 0; u < 4; ++u) pb[u] = *(const u32x4a8*)(src + u * 16 * D_);
  if (tid < 105) {
    const f32x4* pp = (const f32x4*)(part + (size_t)h * 420) + tid;
    f32x4 sum = f32x4{0.f, 0.f, 0.f, 0.f};
#pragma unroll
    for (int p = 0; p < NPART; ++p) sum += pp[(size_t)p * NH * 105];
    *(f32x4*)(red + tid * 4) = sum;
  }
  __syncthreads();
  unsigned t0[4], t1[4];
#pragma unroll
  for (int j2 = 0; j2 < 4; ++j2) {
    float a0[2], a1[2];
#pragma unroll
    for (int jj = 0; jj < 2; ++jj) {
      const int d = 8 * g + 2 * j2 + jj;
      const int dd = d < 20 ? d : 0;
      float v0 = red[dd * 20 + fr];
      float v1 = fr < 4 ? red[dd * 20 + 16 + fr] : red[400 + dd];
      a0[jj] = d < 20 ? v0 : 0.f;
      a1[jj] = (d < 20 && fr <= 4) ? v1 : 0.f;
    }
    t0[j2] = pk2(a0[0], a0[1]);
    t1[j2] = pk2(a1[0], a1[1]);
  }
  const bf16x8 T0 = __builtin_bit_cast(bf16x8, (u32x4{t0[0], t0[1], t0[2], t0[3]}));
  const bf16x8 T1 = __builtin_bit_cast(bf16x8, (u32x4{t1[0], t1[1], t1[2], t1[3]}));
#pragma unroll
  for (int u = 0; u < 4; ++u) {
    u32x4 b = pb[u];
    if (g == 2) { b[2] = 0u; b[3] = 0u; }
    if (g == 3) b = u32x4{0u, 0u, 0u, 0u};
    const bf16x8 bb = __builtin_bit_cast(bf16x8, b);
    f32x4 z4 = f32x4{0.f, 0.f, 0.f, 0.f};
    f32x4 n0 = MFMA16(T0, bb, z4);
    f32x4 n1 = MFMA16(T1, bb, z4);
    float den = __shfl(n1[0], 16 + fr);
    den = rbf(rbf(den) + 1e-6f);
    const float rd = __builtin_amdgcn_rcpf(den);
    const unsigned p01 = pk2(n0[0], n0[1]), p23 = pk2(n0[2], n0[3]);
    const unsigned q01 = pk2(n1[0], n1[1]), q23 = pk2(n1[2], n1[3]);
    u16* dst = o + (size_t)(rc * 256 + wave * 64 + u * 16 + fr) * D_ + h * DH;
    *(u32x2*)(dst + 4 * g) = u32x2{pk2(lo16(p01) * rd, hi16(p01) * rd), pk2(lo16(p23) * rd, hi16(p23) * rd)};
    if (g == 0) *(u32x2*)(dst + 16) = u32x2{pk2(lo16(q01) * rd, hi16(q01) * rd), pk2(lo16(q23) * rd, hi16(q23) * rd)};
  }
}

constexpr int K3_AB = 64 * 128 * 2;
constexpr int K3_BB = 128 * 80 * 2;
constexpr int K3_ST = K3_AB + K3_BB;
constexpr int K3_NSTEP = D_ / 128;
__global__ __launch_bounds__(640) void k3_out(const u16* __restrict__ o, const u16* __restrict__ wo,
                                              u16* __restrict__ out) {
  __shared__ __attribute__((aligned(16))) char smem[4 * K3_ST];
  const int tid = threadIdx.x, lane = tid & 63, fr = lane & 15, g = lane >> 4;
  const int wave = __builtin_amdgcn_readfirstlane(tid >> 6);
  const int bid = blockIdx.x, xcd = bid & 7, loc = bid >> 3;
  const int rt = xcd * 4 + (loc & 3), ct = loc >> 2;
  const int r0 = rt * 64, n0 = ct * 80;
  if (wave == 8) {
    const u16* gp[16];
#pragma unroll
    for (int it = 0; it < 16; ++it) {
      int i = it * 64 + lane;
      int row = i >> 4;
      int c = (i & 15) ^ (row & 15);
      gp[it] = o + (size_t)(r0 + row) * D_ + c * 8;
    }
    auto stage = [&](int t) {
      char* base = smem + (t & 3) * K3_ST;
#pragma unroll
      for (int it = 0; it < 16; ++it) glds16(gp[it] + t * 128, base + it * 1024);
    };
    stage(0); stage(1); stage(2);
    WAITV(32); __builtin_amdgcn_s_barrier(); stage(3);
    WAITV(32); __builtin_amdgcn_s_barrier(); stage(4);
    WAITV(32); __builtin_amdgcn_s_barrier();
    WAITV(16); __builtin_amdgcn_s_barrier();
    WAITV(0); __builtin_amdgcn_s_barrier();
    return;
  }
  if (wave == 9) {
    const u16* gp[20];
#pragma unroll
    for (int it = 0; it < 20; ++it) {
      int i = it * 64 + lane;
      int half = i & 1, rp = (i >> 1) & 7, tile = i >> 4;
      int kb = tile / 5, nt = tile - kb * 5;
      int krow = kb * 8 + (rp ^ (4 * (kb & 1)));
      gp[it] = wo + (size_t)krow * D_ + n0 + nt * 16 + half * 8;
    }
    auto stage = [&](int t) {
      char* base = smem + (t & 3) * K3_ST + K3_AB;
#pragma unroll
      for (int it = 0; it < 20; ++it) glds16(gp[it] + (size_t)t * 128 * D_, base + it * 1024);
    };
    stage(0); stage(1); stage(2);
    WAITV(40); __builtin_amdgcn_s_barrier(); stage(3);
    WAITV(40); __builtin_amdgcn_s_barrier(); stage(4);
    WAITV(40); __builtin_amdgcn_s_barrier();
    WAITV(20); __builtin_amdgcn_s_barrier();
    WAITV(0); __builtin_amdgcn_s_barrier();
    return;
  }
  unsigned pfd;
  {
    const int id = wave * 64 + lane;
    const char* pa;
    if (id < 200) pa = (const char*)wo + (size_t)(loc * 200 + id) * 128;
    else { const int l = id - 200 < 80 ? id - 200 : 79; pa = (const char*)o + (size_t)r0 * (D_ * 2) + (size_t)(ct * 80 + l) * 128; }
    asm volatile("global_load_dword %0, %1, off" : "=v"(pfd) : "v"(pa) : "memory");
  }
  const int rtile = wave & 3, cgp = wave >> 2;
  const int q4 = fr >> 2, p4 = fr & 3;
  const int arow = rtile * 16 + fr;
  const int boff0 = K3_AB + (g * 5 + cgp * 3) * 256 + ((q4 ^ (4 * (g & 1))) * 32) + 8 * p4;
  const int boff1 = K3_AB + (g * 5 + cgp * 3) * 256 + (((4 + q4) ^ (4 * (g & 1))) * 32) + 8 * p4;
  f32x4 acc[3];
#pragma unroll
  for (int n = 0; n < 3; ++n) acc[n] = f32x4{0.f, 0.f, 0.f, 0.f};
#pragma unroll
  for (int t = 0; t < K3_NSTEP; ++t) {
    __builtin_amdgcn_s_barrier();
    const char* buf = smem + (t & 3) * K3_ST;
#pragma unroll
    for (int kk = 0; kk < 4; ++kk) {
      const bf16x8 a = *(const bf16x8*)(buf + arow * 256 + (((4 * kk + g) ^ fr) * 16));
#pragma unroll
      for (int n = 0; n < 3; ++n) {
        if (n < 2 || cgp == 0) {
          s16x4 lo = tr_read(buf + boff0 + kk * 20 * 256 + n * 256), hi = tr_read(buf + boff1 + kk * 20 * 256 + n * 256);
          bf16x8 b = __builtin_shufflevector(lo, hi, 0, 1, 2, 3, 4, 5, 6, 7);
          acc[n] = MFMA16(b, a, acc[n]);
        }
      }
    }
  }
  asm volatile("s_waitcnt vmcnt(0)" :: "v"(pfd) : "memory");
  u16* dst = out + (size_t)(r0 + arow) * D_ + n0 + cgp * 48 + 4 * g;
#pragma unroll
  for (int n = 0; n < 3; ++n)
    if (n < 2 || cgp == 0) *(u32x2*)(dst + 16 * n) = u32x2{pk2(acc[n][0], acc[n][1]), pk2(acc[n][2], acc[n][3])};
}

extern "C" void kernel_launch(void* const* d_in, const int* in_sizes, int n_in,
                              void* d_out, int out_size, void* d_ws, size_t ws_size,
                              hipStream_t stream) {
  const u16* x = (const u16*)d_in[0];
  const u16* w = (const u16*)d_in[1];
  const u16* wo = (const u16*)d_in[2];
  char* ws = (char*)d_ws;
  u16* pq = (u16*)ws;
  float* part = (float*)(ws + 2621440);
  u16* o = (u16*)(ws + 2621440 + 860160);
  k1_qkv<<<192, 640, 0, stream>>>(x, w, pq, part);
  k2_o<<<256, 256, 0, stream>>>(pq, part, o);
  k3_out<<<256, 640, 0, stream>>>(o, wo, (u16*)d_out);
}
```
